```python
import torch

def _softmax(x, axis=-1):
    return torch.softmax(x, dim=axis)
def _gelu(x):
    return torch.nn.functional.gelu(x, approximate="tanh")
def _silu(x):
    return torch.nn.functional.silu(x)
def _sigmoid(x):
    return torch.sigmoid(x)
def _layernorm(x, axis=-1, eps=1e-5):
    m = x.mean(dim=axis, keepdim=True)
    v = x.var(dim=axis, keepdim=True, unbiased=False)
    return (x - m) / torch.sqrt(v + eps)
def _scatter_add(tgt, idx, src, axis=0):
    return tgt.clone().index_add_(axis, idx, src)
def _segment_sum(src, seg, n):
    out = torch.zeros((n,) + src.shape[1:], dtype=src.dtype)
    return out.index_add_(0, seg, src)
def _pad(x, pw):
    flat = tuple(v for pair in reversed(pw) for v in pair)
    return torch.nn.functional.pad(x, flat)

kernel_name = 'concat_heads__sliding_block_cached__mha_block_cached__decode'

def setup_inputs():

    _w = (lambda a: (lambda t: t.to(torch.bfloat16) if t.is_floating_point() else t)(torch.as_tensor(a)))
    def _A_setup():
        B, Sq, D = 1, 1, 512
        Hq, Hkv, Sk, Dh = 32, 8, 16384, 128
        assert Hq % Hkv == 0, (Hq, Hkv)
        return {
            "x": _w((lambda *s: torch.randn(*s, dtype=torch.bfloat16) * 0.8)(B, Sq, D)),
            "k_cache": _w((lambda *s: torch.randn(*s, dtype=torch.bfloat16) * 1.6)(B, Hkv, Sk, Dh)),
            "v_cache": _w((lambda *s: torch.randn(*s, dtype=torch.bfloat16) * 0.2)(B, Hkv, Sk, Dh)),
            "wq": _w((lambda *s: torch.randn(*s, dtype=torch.bfloat16) * 1.6)(D, Hq * Dh) * (D ** -0.5)),
            "wo": _w((lambda *s: torch.randn(*s, dtype=torch.bfloat16) * 0.2)(Hq * Dh, D) * ((Hq * Dh) ** -0.5)),
        }
    _A_in = _A_setup()

    def _B_setup():
        B, Sq, D = 1, 1, 512
        H, Sk, Dh = 32, 16384, 128
        return {
            "x": _w((lambda *s: torch.randn(*s, dtype=torch.bfloat16) * 0.8)(B, Sq, D)),
            "k_cache": _w((lambda *s: torch.randn(*s, dtype=torch.bfloat16) * 1.6)(B, H, Sk, Dh)),
            "v_cache": _w((lambda *s: torch.randn(*s, dtype=torch.bfloat16) * 0.2)(B, H, Sk, Dh)),
            "wq": _w((lambda *s: torch.randn(*s, dtype=torch.bfloat16) * 1.6)(D, H * Dh) * (D ** -0.5)),
            "wo": _w((lambda *s: torch.randn(*s, dtype=torch.bfloat16) * 0.2)(H * Dh, D) * ((H * Dh) ** -0.5)),
        }
    _B_in = _B_setup()

    return {
        "A_x": _A_in["x"],
        "A_k_cache": _A_in["k_cache"],
        "A_v_cache": _A_in["v_cache"],
        "A_wq": _A_in["wq"],
        "A_wo": _A_in["wo"],
        "B_x": _B_in["x"],
        "B_k_cache": _B_in["k_cache"],
        "B_v_cache": _B_in["v_cache"],
        "B_wq": _B_in["wq"],
        "B_wo": _B_in["wo"],
    }

def reference(A_x, A_k_cache, A_v_cache, A_wq, A_wo, B_x, B_k_cache, B_v_cache, B_wq, B_wo):
    def _A_ref(x, k_cache, v_cache, wq, wo):
        B, Sq, D = x.shape
        Hq, Dh = 32, 128
        Hkv, Sk = k_cache.shape[1], k_cache.shape[2]
        W = min(4096, max(256, Sk // 4))
        rep = Hq // Hkv
        q = (lambda x, *d: x.permute(*d))((x @ wq).reshape(B, Sq, Hq, Dh), 0, 2, 1, 3)
        k = (lambda x, n, axis: torch.repeat_interleave(x, n, dim=axis))(k_cache[:, :, Sk - W:, :], rep, axis=1)
        v = (lambda x, n, axis: torch.repeat_interleave(x, n, dim=axis))(v_cache[:, :, Sk - W:, :], rep, axis=1)
        scores = torch.einsum("bhqd,bhkd->bhqk", q, k) * (Dh ** -0.5)
        p = _softmax(scores, axis=-1)
        o = torch.einsum("bhqk,bhkd->bhqd", p, v)
        return (lambda x, *d: x.permute(*d))(o, 0, 2, 1, 3).reshape(B, Sq, Hq * Dh) @ wo

    def _B_ref(x, k_cache, v_cache, wq, wo):
        B, Sq, D = x.shape
        H, Dh = 32, 128
        q = (lambda x, *d: x.permute(*d))((x @ wq).reshape(B, Sq, H, Dh), 0, 2, 1, 3)
        scores = torch.einsum("bhqd,bhkd->bhqk", q, k_cache) * (Dh ** -0.5)
        p = _softmax(scores, axis=-1)
        o = torch.einsum("bhqk,bhkd->bhqd", p, v_cache)
        return (lambda x, *d: x.permute(*d))(o, 0, 2, 1, 3).reshape(B, Sq, H * Dh) @ wo

    a = _A_ref(A_x, A_k_cache, A_v_cache, A_wq, A_wo)
    b = _B_ref(B_x, B_k_cache, B_v_cache, B_wq, B_wo)
    return torch.concatenate([a, b], axis=-1)
```

```cpp
#include <hip/hip_runtime.h>
#include <hip/hip_bf16.h>
#include <stdint.h>
#include <utility>

#define GAS __attribute__((address_space(1)))
typedef unsigned v4u __attribute__((ext_vector_type(4)));
typedef unsigned v2u __attribute__((ext_vector_type(2)));
typedef float f32x4 __attribute__((ext_vector_type(4)));
typedef float f32x2 __attribute__((ext_vector_type(2)));
typedef short bf16x8 __attribute__((ext_vector_type(8)));
typedef __bf16 bf16x2_t __attribute__((ext_vector_type(2)));
typedef __amdgpu_buffer_rsrc_t rsrc_t;

namespace {

constexpr int AUX_NT = 2, AUX_SC1 = 16;
constexpr int RSRC_W3 = 0x00020000;
constexpr float C2 = 0.08838834764831845f * 1.4426950408889634f;
constexpr int SK = 16384, WIN0 = 16384 - 4096;
constexpr int SA = 4;
constexpr int NSLOT = 17;
constexpr unsigned SPIN_CAP = 1u << 18;

constexpr unsigned WS_GEN = 0;
constexpr unsigned WS_QA = 4096;
constexpr unsigned PA_REC = 130 * 8;
constexpr unsigned WS_PA = WS_QA + 8 * 256 * 8;
constexpr unsigned WS_MLB = WS_PA + 8 * 32 * 4 * PA_REC;
constexpr unsigned WS_YA = WS_MLB + 4096;
constexpr unsigned WS_YB = WS_YA + (1u << 20);
constexpr unsigned WS_END = WS_YB + (1u << 20);

constexpr int L_QPART = 0;
constexpr int L_QAPART = L_QPART + 8 * 128;
constexpr int L_QFIN = L_QAPART + 8 * 64;
constexpr int L_PARTA = L_QFIN + 64;
constexpr int L_PARTB = L_PARTA + 8 * 4 * 132;
constexpr int L_MLB = L_PARTB + 8 * 128;
constexpr int L_YPART = L_MLB + 16;
constexpr int L_RED = L_YPART + 8 * 512;
constexpr int L_END = L_RED + 16;

__device__ __forceinline__ rsrc_t mk_rsrc(const void* p, unsigned bytes) {
  return __builtin_amdgcn_make_buffer_rsrc(const_cast<void*>(p), 0, bytes, RSRC_W3);
}
template <int AUX> __device__ __forceinline__ v4u ld128(rsrc_t rs, unsigned voff, unsigned soff) {
  return __builtin_amdgcn_raw_buffer_load_b128(rs, voff, soff, AUX);
}
__device__ __forceinline__ f32x4 mfma(v4u a, v4u b, f32x4 c) {
  return __builtin_amdgcn_mfma_f32_16x16x32_bf16(__builtin_bit_cast(bf16x8, a), __builtin_bit_cast(bf16x8, b), c, 0, 0, 0);
}
__device__ __forceinline__ unsigned cvtpk(float lo, float hi) {
  f32x2 v = {lo, hi};
  return __builtin_bit_cast(unsigned, __builtin_convertvector(v, bf16x2_t));
}
template <int CTRL> __device__ __forceinline__ float dppf(float x) {
  return __builtin_bit_cast(float, __builtin_amdgcn_mov_dpp(__builtin_bit_cast(int, x), CTRL, 0xf, 0xf, true));
}
template <int CTRL, int RM, int BM> __device__ __forceinline__ unsigned dppu(unsigned old, unsigned x) {
  return (unsigned)__builtin_amdgcn_update_dpp((int)old, (int)x, CTRL, RM, BM, false);
}
constexpr int XOR1 = 0xB1, XOR2 = 0x4E, HMIRROR = 0x141, MIRROR = 0x140, ROR8 = 0x128, ROR4 = 0x124, BCAST = 0x150;
__device__ __forceinline__ float xrow_max(float x) {
  auto s = __builtin_amdgcn_permlane16_swap(__float_as_uint(x), __float_as_uint(x), false, false);
  x = fmaxf(__uint_as_float(s[0]), __uint_as_float(s[1]));
  auto t = __builtin_amdgcn_permlane32_swap(__float_as_uint(x), __float_as_uint(x), false, false);
  return fmaxf(__uint_as_float(t[0]), __uint_as_float(t[1]));
}
__device__ __forceinline__ float xrow_sum(float x) {
  auto s = __builtin_amdgcn_permlane16_swap(__float_as_uint(x), __float_as_uint(x), false, false);
  x = __uint_as_float(s[0]) + __uint_as_float(s[1]);
  auto t = __builtin_amdgcn_permlane32_swap(__float_as_uint(x), __float_as_uint(x), false, false);
  return __uint_as_float(t[0]) + __uint_as_float(t[1]);
}
__device__ __forceinline__ v4u andm(unsigned rep, const v4u& m) { return v4u{rep & m.x, rep & m.y, rep & m.z, rep & m.w}; }
__device__ __forceinline__ unsigned split_rep(float x, bool take_lo) {
  const unsigned hi = cvtpk(x, x);
  const float r = x - __uint_as_float(hi & 0xffff0000u);
  const unsigned lo = cvtpk(r, r);
  return take_lo ? lo : hi;
}
#define SB() __builtin_amdgcn_sched_barrier(0)
#define MEMFENCE() asm volatile("" ::: "memory")
#define PIN(x) asm volatile("" : "+v"(x) :: "memory")

template <typename F, int... I>
__device__ __forceinline__ void run_slots(F& f, std::integer_sequence<int, I...>) { (f(std::integral_constant<int, I>{}), ...); }

struct State { float m, l; f32x4 acc; };

__device__ __forceinline__ void qk_part(const v4u (&K)[4], const v4u (&Bq)[4], f32x4& S) {
  S = f32x4{0.f, 0.f, 0.f, 0.f};
#pragma unroll
  for (int u = 0; u < 4; ++u) S = mfma(K[u], Bq[u], S);
}
__device__ __forceinline__ void sm_pv(const f32x4& S, const v4u (&V)[4], const v4u& mask, int n, State& st) {
  const float s01 = (n & 1) ? S[1] : S[0], s23 = (n & 1) ? S[3] : S[2];
  float s = (n & 2) ? s23 : s01;
  s += dppf<XOR1>(s);
  s += dppf<XOR2>(s);
  float tm = fmaxf(s, dppf<ROR4>(s));
  tm = fmaxf(tm, dppf<ROR8>(tm));
  tm = xrow_max(tm);
  const float mn = fmaxf(st.m, tm);
  const float alpha = __builtin_amdgcn_exp2f((st.m - mn) * C2);
  const float p = __builtin_amdgcn_exp2f((s - mn) * C2);
  st.m = mn;
  st.l = fmaf(st.l, alpha, p);
  st.acc *= alpha;
  const unsigned pk = split_rep(p, (n & 2) != 0);
  unsigned rep;
#define PV_STEP(u) rep = dppu<BCAST + 4 * (u), 0xf, 0x3>(pk, pk); rep = dppu<BCAST + 4 * (u) + 2, 0xf, 0xc>(rep, pk); \
  st.acc = mfma(V[u], andm(rep, mask), st.acc);
  PV_STEP(0) PV_STEP(1) PV_STEP(2) PV_STEP(3)
#undef PV_STEP
}
__device__ __forceinline__ void finish_state(State& st) {
  float l = st.l;
  l += dppf<ROR4>(l);
  l += dppf<ROR8>(l);
  st.l = xrow_sum(l);
#pragma unroll
  for (int i = 0; i < 4; ++i) st.acc[i] += dppf<ROR8>(st.acc[i]);
}
__device__ __forceinline__ int opos(int d) { return (d >> 4) * 16 + (d & 3) * 4 + ((d >> 2) & 3); }
__device__ __forceinline__ bool tag2(const v4u& x, unsigned tag) { return x.y == tag && x.w == tag; }

__global__ void __launch_bounds__(512, 2)
fused_decode(const uint16_t* __restrict__ xA, const uint16_t* __restrict__ kA, const uint16_t* __restrict__ vA,
             const uint16_t* __restrict__ wqA, const uint16_t* __restrict__ woA,
             const uint16_t* __restrict__ xB, const uint16_t* __restrict__ kB, const uint16_t* __restrict__ vB,
             const uint16_t* __restrict__ wqB, const uint16_t* __restrict__ woB,
             uint16_t* __restrict__ out, unsigned char* __restrict__ ws) {
  __shared__ __attribute__((aligned(16))) float lds[L_END];
  const int tid = threadIdx.x, lane = tid & 63, wv = __builtin_amdgcn_readfirstlane(tid >> 6);
  const int g = lane >> 4, n = lane & 15;
  const int w = blockIdx.x, x8 = w & 7, j = w >> 3;
  const int hB = 4 * x8 + (j & 3), sB = j >> 2;

  const rsrc_t rKB = mk_rsrc(kB + ((size_t)hB * SK + sB * 2048) * 128, 2048 * 256);
  const rsrc_t rVB = mk_rsrc(vB + ((size_t)hB * SK + sB * 2048) * 128, 2048 * 256);
  const rsrc_t rKA = mk_rsrc(kA + ((size_t)x8 * SK + WIN0 + j * 128) * 128, 128 * 256);
  const rsrc_t rVA = mk_rsrc(vA + ((size_t)x8 * SK + WIN0 + j * 128) * 128, 128 * 256);
  const rsrc_t rWQB = mk_rsrc(wqB, 512 * 8192);
  const rsrc_t rWQA = mk_rsrc(wqA, 512 * 8192);
  const rsrc_t rWOB = mk_rsrc(woB, 4096 * 1024);
  const rsrc_t rWOA = mk_rsrc(woA, 4096 * 1024);
  const rsrc_t rXB = mk_rsrc(xB, 1024);
  const rsrc_t rWS = mk_rsrc(ws, WS_END);

  const unsigned koff = (n >> 2) * 256 + g * 64 + (n & 3) * 16, voff = lane * 16;
  const unsigned wvoff = wv * 4096;

  v4u xb[2];
  xb[0] = ld128<0>(rXB, (64 * wv + 16 * g) * 2, 0);
  xb[1] = ld128<0>(rXB, (64 * wv + 16 * g) * 2 + 16, 0);
  v4u wq[16], qa[8], g2[8], woa[16], wob[16], ofr;
#pragma unroll
  for (int t = 0; t < 16; ++t) wq[t] = ld128<0>(rWQB, g * 16 * 8192 + n * 16, (64 * wv + t) * 8192 + hB * 256);
  SB();
  v4u K[2][4], V[2][4];
  auto issueK = [&](int s) __attribute__((always_inline)) {
    const bool isA = s == SA;
    const int tb = s < SA ? s : s - 1;
#pragma unroll
    for (int u = 0; u < 4; ++u) K[s & 1][u] = ld128<AUX_NT>(isA ? rKA : rKB, koff + u * 1024, wvoff + (isA ? 0 : tb * 32768));
  };
  auto issueV = [&](int s) __attribute__((always_inline)) {
    const bool isA = s == SA;
    const int tb = s < SA ? s : s - 1;
#pragma unroll
    for (int u = 0; u < 4; ++u) V[s & 1][u] = ld128<AUX_NT>(isA ? rVA : rVB, voff + u * 1024, wvoff + (isA ? 0 : tb * 32768));
  };
  issueK(0); issueV(0); issueK(1); issueV(1);
  SB();
  v4u wa[2];
  float xa[2];
#pragma unroll
  for (int t = 0; t < 2; ++t) {
    wa[t] = ld128<0>(rWQA, (lane >> 1) * 8192 + (lane & 1) * 16, (64 * wv + 32 * t) * 8192 + (512 * x8 + 16 * j) * 2);
    xa[t] = __uint_as_float((unsigned)xA[64 * wv + 32 * t + (lane >> 1)] << 16);
  }
  const unsigned tag = *(const GAS unsigned*)(ws + WS_GEN) + 1u;
  SB();

  v4u mask;
  {
    const unsigned hm = (n & 1) ? 0xffff0000u : 0x0000ffffu;
    const int q = (n & 7) >> 1;
    mask = v4u{q == 0 ? hm : 0u, q == 1 ? hm : 0u, q == 2 ? hm : 0u, q == 3 ? hm : 0u};
  }
  {
    f32x4 D = {0.f, 0.f, 0.f, 0.f};
#pragma unroll
    for (int t = 0; t < 16; ++t) {
      const unsigned xp = xb[t >> 3][(t >> 1) & 3];
      const unsigned rep = __builtin_amdgcn_perm(xp, xp, (t & 1) ? 0x03020302u : 0x01000100u);
      D = mfma(wq[t], andm(rep, mask), D);
    }
    if (n < 8) *(f32x4*)&lds[L_QPART + wv * 128 + (g * 8 + n) * 4] = D;
    float a[8];
#pragma unroll
    for (int k = 0; k < 4; ++k) {
      a[2 * k] = xa[0] * __uint_as_float(wa[0][k] << 16) + xa[1] * __uint_as_float(wa[1][k] << 16);
      a[2 * k + 1] = xa[0] * __uint_as_float(wa[0][k] & 0xffff0000u) + xa[1] * __uint_as_float(wa[1][k] & 0xffff0000u);
    }
#pragma unroll
    for (int k = 0; k < 8; ++k) { a[k] += dppf<XOR2>(a[k]); a[k] += dppf<ROR4>(a[k]); a[k] += dppf<ROR8>(a[k]); }
    if (n < 2) {
      *(f32x4*)&lds[L_QAPART + (wv * 4 + g) * 16 + n * 8] = f32x4{a[0], a[1], a[2], a[3]};
      *(f32x4*)&lds[L_QAPART + (wv * 4 + g) * 16 + n * 8 + 4] = f32x4{a[4], a[5], a[6], a[7]};
    }
  }
  __syncthreads();
  if (tid < 128) {
    float q = 0.f;
#pragma unroll
    for (int k = 0; k < 8; ++k) q += lds[L_QPART + k * 128 + tid];
    const int c = 32 * (tid >> 5) + 8 * (tid & 3) + ((tid >> 2) & 7);
    ((uint16_t*)&lds[L_QFIN])[c] = (uint16_t)(cvtpk(q, q) & 0xffffu);
  } else if (wv == 2) {
    float q = 0.f;
#pragma unroll
    for (int k = 0; k < 8; ++k) q += lds[L_QAPART + (g * 8 + k) * 16 + n];
    q = xrow_sum(q);
    const float q1 = dppf<XOR1>(q);
    if (lane < 16 && !(lane & 1))
      __builtin_amdgcn_raw_buffer_store_b64(v2u{cvtpk(q, q1), tag}, rWS, WS_QA + (x8 * 256 + 8 * j + (lane >> 1)) * 8, 0, AUX_SC1);
  }
  __syncthreads();
  const v4u qB = *(const v4u*)&lds[L_QFIN + (4 * g + (n & 3)) * 4];
  v4u Bq[4];
  auto make_bq = [&](const v4u& q) __attribute__((always_inline)) {
#pragma unroll
    for (int u = 0; u < 4; ++u) Bq[u] = ((n >> 2) == u) ? q : v4u{0u, 0u, 0u, 0u};
  };
  make_bq(qB);
  State st;
  st.m = -INFINITY; st.l = 0.f; st.acc = f32x4{0.f, 0.f, 0.f, 0.f};

  auto slot = [&](auto S_) __attribute__((always_inline)) {
    constexpr int s = decltype(S_)::value;
    if constexpr (s != SA) {
      f32x4 S;
      qk_part(K[s & 1], Bq, S);
      PIN(S);
      SB();
      if constexpr (s + 2 < NSLOT) issueK(s + 2);
      SB();
      sm_pv(S, V[s & 1], mask, n, st);
      PIN(st.acc); PIN(st.l);
      SB();
      if constexpr (s + 2 < NSLOT) issueV(s + 2);
      SB();
    } else {
      for (unsigned spins = 0;; ++spins) {
        bool ok = true;
#pragma unroll
        for (int k = 0; k < 8; ++k) ok &= tag2(qa[k], tag);
        if (__all(ok) || spins > SPIN_CAP) break;
        __builtin_amdgcn_s_sleep(2);
        MEMFENCE();
#pragma unroll
        for (int k = 0; k < 8; ++k)
          qa[k] = ld128<AUX_SC1>(rWS, WS_QA + (x8 * 256 + (k >> 1) * 64 + (4 * g + (n & 3)) * 4 + (k & 1) * 2) * 8, 0);
      }
#pragma unroll
      for (int hd = 0; hd < 4; ++hd) {
        make_bq(v4u{qa[2 * hd].x, qa[2 * hd].z, qa[2 * hd + 1].x, qa[2 * hd + 1].z});
        State sa;
        sa.m = -INFINITY; sa.l = 0.f; sa.acc = f32x4{0.f, 0.f, 0.f, 0.f};
        f32x4 S;
        qk_part(K[s & 1], Bq, S);
        sm_pv(S, V[s & 1], mask, n, sa);
        finish_state(sa);
        float* rec = &lds[L_PARTA + (wv * 4 + hd) * 132];
        if (n < 8) {
#pragma unroll
          for (int i = 0; i < 4; ++i) rec[4 + 32 * g + 8 * i + n] = sa.acc[i];
        }
        if (lane == 0) { rec[0] = sa.m; rec[1] = sa.l; }
        MEMFENCE();
      }
      SB();
      issueK(s + 2); issueV(s + 2);
      SB();
      make_bq(qB);
      __syncthreads();
      {
        const int hd = wv >> 1, d = 64 * (wv & 1) + lane;
        float M = -INFINITY;
#pragma unroll
        for (int k = 0; k < 8; ++k) M = fmaxf(M, lds[L_PARTA + (k * 4 + hd) * 132]);
        float o = 0.f, L = 0.f;
#pragma unroll
        for (int k = 0; k < 8; ++k) {
          const float* rec = &lds[L_PARTA + (k * 4 + hd) * 132];
          const float f = __builtin_amdgcn_exp2f((rec[0] - M) * C2);
          o = fmaf(f, rec[4 + d], o);
          L = fmaf(f, rec[1], L);
        }
        const unsigned base = WS_PA + ((x8 * 32 + j) * 4 + hd) * PA_REC;
        __builtin_amdgcn_raw_buffer_store_b64(v2u{__float_as_uint(o), tag}, rWS, base + 16 + d * 8, 0, AUX_SC1);
        if (d == 0) __builtin_amdgcn_raw_buffer_store_b128(v4u{__float_as_uint(M), tag, __float_as_uint(L), tag}, rWS, base, 0, AUX_SC1);
      }
      SB();
    }
    if constexpr (s == SA - 2) {
#pragma unroll
      for (int k = 0; k < 8; ++k)
        qa[k] = ld128<AUX_SC1>(rWS, WS_QA + (x8 * 256 + (k >> 1) * 64 + (4 * g + (n & 3)) * 4 + (k & 1) * 2) * 8, 0);
      SB();
    }
    if constexpr (s == 8) if (wv == 7) {
      const unsigned base = WS_PA + ((x8 * 32 + (lane >> 3)) * 4 + (j >> 3)) * PA_REC;
#pragma unroll
      for (int k = 0; k < 4; ++k) {
        g2[k] = ld128<AUX_SC1>(rWS, base + k * 8 * 4 * PA_REC, 0);
        g2[4 + k] = ld128<AUX_SC1>(rWS, base + k * 8 * 4 * PA_REC + 16 + (16 * (j & 7) + 2 * (lane & 7)) * 8, 0);
      }
      SB();
    }
    if constexpr (s == 9) if (wv == 7) {
      const unsigned base = WS_PA + ((x8 * 32 + (lane >> 3)) * 4 + (j >> 3)) * PA_REC;
      for (unsigned spins = 0;; ++spins) {
        bool ok = true;
#pragma unroll
        for (int k = 0; k < 8; ++k) ok &= tag2(g2[k], tag);
        if (__all(ok) || spins > SPIN_CAP) break;
        __builtin_amdgcn_s_sleep(2);
        MEMFENCE();
#pragma unroll
        for (int k = 0; k < 4; ++k) {
          g2[k] = ld128<AUX_SC1>(rWS, base + k * 8 * 4 * PA_REC, 0);
          g2[4 + k] = ld128<AUX_SC1>(rWS, base + k * 8 * 4 * PA_REC + 16 + (16 * (j & 7) + 2 * (lane & 7)) * 8, 0);
        }
      }
      float M = -INFINITY;
#pragma unroll
      for (int k = 0; k < 4; ++k) M = fmaxf(M, __uint_as_float(g2[k].x));
      M = fmaxf(M, dppf<ROR8>(M));
      M = xrow_max(M);
      float L = 0.f, a0 = 0.f, a1 = 0.f;
#pragma unroll
      for (int k = 0; k < 4; ++k) {
        const float f = __builtin_amdgcn_exp2f((__uint_as_float(g2[k].x) - M) * C2);
        L = fmaf(f, __uint_as_float(g2[k].z), L);
        a0 = fmaf(f, __uint_as_float(g2[4 + k].x), a0);
        a1 = fmaf(f, __uint_as_float(g2[4 + k].z), a1);
      }
      L += dppf<ROR8>(L); a0 += dppf<ROR8>(a0); a1 += dppf<ROR8>(a1);
      L = xrow_sum(L); a0 = xrow_sum(a0); a1 = xrow_sum(a1);
      const float v = ((g & 1) ? a1 : a0) / L;
      const unsigned vu = __float_as_uint(v);
      unsigned xr;
#define OA_STEP(rg) xr = dppu<BCAST + 2 * (rg), 0x3, 0xf>(vu, vu); xr = dppu<BCAST + 2 * (rg) + 1, 0xc, 0xf>(xr, vu); \
      Bq0[rg] = split_rep(__uint_as_float(xr), n >= 8);
      unsigned Bq0[4];
      OA_STEP(0) OA_STEP(1) OA_STEP(2) OA_STEP(3)
#undef OA_STEP
      ofr = v4u{Bq0[0], Bq0[1], Bq0[2], Bq0[3]};
#pragma unroll
      for (int k = 0; k < 16; ++k)
        woa[k] = ld128<AUX_NT>(rWOA, g * 1024 + n * 16, (512 * x8 + 16 * j + 4 * (k >> 2)) * 1024 + (k & 3) * 256);
      SB();
    }
    if constexpr (s == 10) if (wv == 7) {
      f32x4 Y[4];
#pragma unroll
      for (int cb = 0; cb < 4; ++cb) Y[cb] = f32x4{0.f, 0.f, 0.f, 0.f};
#pragma unroll
      for (int rg = 0; rg < 4; ++rg) {
        const v4u fr = andm(ofr[rg], mask);
#pragma unroll
        for (int cb = 0; cb < 4; ++cb) Y[cb] = mfma(woa[rg * 4 + cb], fr, Y[cb]);
      }
#pragma unroll
      for (int cb = 0; cb < 4; ++cb) {
        float y[4], yn[4];
#pragma unroll
        for (int i = 0; i < 4; ++i) { y[i] = Y[cb][i] + dppf<ROR8>(Y[cb][i]); yn[i] = dppf<XOR1>(y[i]); }
        const int i = (n & 1) + ((n >> 3) << 1);
        const float own = (i & 2) ? ((i & 1) ? y[3] : y[2]) : ((i & 1) ? y[1] : y[0]);
        const float nb = (i & 2) ? ((i & 1) ? yn[3] : yn[2]) : ((i & 1) ? yn[1] : yn[0]);
        const float e0 = (n & 1) ? nb : own, e1 = (n & 1) ? own : nb;
        const int pair = 64 * cb + 16 * g + 4 * i + ((n & 7) >> 1);
        __builtin_amdgcn_raw_buffer_store_b128(v4u{__float_as_uint(e0), tag, __float_as_uint(e1), tag}, rWS,
                                               WS_YA + (pair * 256 + w) * 16, 0, AUX_SC1);
      }
      SB();
    }
    if constexpr (s == 11) {
#pragma unroll
      for (int k = 0; k < 16; ++k)
        wob[k] = ld128<0>(rWOB, g * 1024 + n * 16, (hB * 128 + 16 * wv + 4 * (k >> 2)) * 1024 + (k & 3) * 256);
      SB();
    }
  };
  run_slots(slot, std::make_integer_sequence<int, NSLOT>{});

  finish_state(st);
  if (n < 8) {
#pragma unroll
    for (int i = 0; i < 4; ++i) lds[L_PARTB + wv * 128 + opos(32 * g + 8 * i + n)] = st.acc[i];
  }
  if (lane == 0) { lds[L_MLB + 2 * wv] = st.m; lds[L_MLB + 2 * wv + 1] = st.l; }
  __syncthreads();
  {
    float M = -INFINITY;
#pragma unroll
    for (int k = 0; k < 8; ++k) M = fmaxf(M, lds[L_MLB + 2 * k]);
    float L = 0.f;
    f32x4 o4 = {0.f, 0.f, 0.f, 0.f};
#pragma unroll
    for (int k = 0; k < 8; ++k) {
      const float f = __builtin_amdgcn_exp2f((lds[L_MLB + 2 * k] - M) * C2);
      L = fmaf(f, lds[L_MLB + 2 * k + 1], L);
      o4 += f * *(const f32x4*)&lds[L_PARTB + k * 128 + 16 * wv + 4 * g];
    }
    if (tid == 0)
      __builtin_amdgcn_raw_buffer_store_b128(v4u{__float_as_uint(M), tag, __float_as_uint(L), tag}, rWS, WS_MLB + (hB * 8 + sB) * 16, 0, AUX_SC1);
    f32x4 Y[4];
#pragma unroll
    for (int cb = 0; cb < 4; ++cb) Y[cb] = f32x4{0.f, 0.f, 0.f, 0.f};
#pragma unroll
    for (int rg = 0; rg < 4; ++rg) {
      const v4u fr = andm(split_rep(o4[rg], n >= 8), mask);
#pragma unroll
      for (int cb = 0; cb < 4; ++cb) Y[cb] = mfma(wob[rg * 4 + cb], fr, Y[cb]);
    }
#pragma unroll
    for (int cb = 0; cb < 4; ++cb) {
#pragma unroll
      for (int i = 0; i < 4; ++i) Y[cb][i] += dppf<ROR8>(Y[cb][i]);
      if (n < 8) *(f32x4*)&lds[L_YPART + wv * 512 + ((cb * 4 + g) * 8 + n) * 4] = Y[cb];
    }
  }
  __syncthreads();
  if (tid < 256) {
    const int c = 2 * tid;
    const int idx = (((c >> 7) * 4 + ((c >> 5) & 3)) * 8 + (c & 7)) * 4 + ((c >> 3) & 3);
    float y0 = 0.f, y1 = 0.f;
#pragma unroll
    for (int k = 0; k < 8; ++k) { y0 += lds[L_YPART + k * 512 + idx]; y1 += lds[L_YPART + k * 512 + idx + 4]; }
    __builtin_amdgcn_raw_buffer_store_b128(v4u{__float_as_uint(y0), tag, __float_as_uint(y1), tag}, rWS,
                                           WS_YB + (tid * 256 + hB * 8 + sB) * 16, 0, AUX_SC1);
  }
  {
    const bool isB = wv < 4;
    const unsigned yoff = (isB ? WS_YB : WS_YA) + (w * 256 + (tid & 255)) * 16;
    v4u y, ml = v4u{0u, tag, 0u, tag};
    for (unsigned spins = 0;; ++spins) {
      MEMFENCE();
      y = ld128<AUX_SC1>(rWS, yoff, 0);
      if (isB) ml = ld128<AUX_SC1>(rWS, WS_MLB + tid * 16, 0);
      if (__all(tag2(y, tag) && tag2(ml, tag)) || spins > SPIN_CAP) break;
      __builtin_amdgcn_s_sleep(1);
    }
    float v0 = __uint_as_float(y.x), v1 = __uint_as_float(y.z);
    if (isB) {
      const float m = __uint_as_float(ml.x);
      float M = fmaxf(m, dppf<XOR1>(m));
      M = fmaxf(M, dppf<XOR2>(M));
      M = fmaxf(M, dppf<HMIRROR>(M));
      const float f = __builtin_amdgcn_exp2f((m - M) * C2);
      float L = f * __uint_as_float(ml.z);
      L += dppf<XOR1>(L); L += dppf<XOR2>(L); L += dppf<HMIRROR>(L);
      const float c = f / L;
      v0 *= c; v1 *= c;
    }
    v0 += dppf<XOR1>(v0); v0 += dppf<XOR2>(v0); v0 += dppf<ROR4>(v0); v0 += dppf<ROR8>(v0); v0 = xrow_sum(v0);
    v1 += dppf<XOR1>(v1); v1 += dppf<XOR2>(v1); v1 += dppf<ROR4>(v1); v1 += dppf<ROR8>(v1); v1 = xrow_sum(v1);
    if (lane == 0) { lds[L_RED + 2 * wv] = v0; lds[L_RED + 2 * wv + 1] = v1; }
  }
  __syncthreads();
  if (lane == 0 && (wv == 0 || wv == 4)) {
    const float* r = &lds[L_RED + 2 * wv];
    const float s0 = (r[0] + r[2]) + (r[4] + r[6]), s1 = (r[1] + r[3]) + (r[5] + r[7]);
    *(unsigned*)(out + (wv == 0 ? 512 : 0) + 2 * w) = cvtpk(s0, s1);
    if (w == 0 && wv == 0) *(GAS unsigned*)(ws + WS_GEN) = tag;
  }
}

}

extern "C" void kernel_launch(void* const* d_in, const int* in_sizes, int n_in,
                              void* d_out, int out_size, void* d_ws, size_t ws_size,
                              hipStream_t stream) {
  (void)in_sizes; (void)n_in; (void)out_size; (void)ws_size;
  fused_decode<<<256, 512, 0, stream>>>(
      (const uint16_t*)d_in[0], (const uint16_t*)d_in[1], (const uint16_t*)d_in[2], (const uint16_t*)d_in[3], (const uint16_t*)d_in[4],
      (const uint16_t*)d_in[5], (const uint16_t*)d_in[6], (const uint16_t*)d_in[7], (const uint16_t*)d_in[8], (const uint16_t*)d_in[9],
      (uint16_t*)d_out, (unsigned char*)d_ws);
}
```

```cpp
#define LOCKSTEP 2
#define DIST 4
#include <hip/hip_runtime.h>
#include <hip/hip_bf16.h>
#include <stdint.h>
#include <utility>

#define GAS __attribute__((address_space(1)))
typedef unsigned v4u __attribute__((ext_vector_type(4)));
typedef unsigned v2u __attribute__((ext_vector_type(2)));
typedef float f32x4 __attribute__((ext_vector_type(4)));
typedef float f32x2 __attribute__((ext_vector_type(2)));
typedef short bf16x8 __attribute__((ext_vector_type(8)));
typedef __bf16 bf16x2_t __attribute__((ext_vector_type(2)));
typedef __amdgpu_buffer_rsrc_t rsrc_t;

namespace {

constexpr int AUX_NT = 2, AUX_SC1 = 16;
constexpr int RSRC_W3 = 0x00020000;
constexpr float SCALE = 0.08838834764831845f;
constexpr float C2 = 1.4426950408889634f;
constexpr int SK = 16384, WIN0 = 16384 - 4096;
constexpr int NSLOT = 17;
constexpr int PRE = 5;
constexpr int next_unit(int u) { return (u + DIST >= PRE && u + DIST < 2 * NSLOT) ? u + DIST : -1; }
#define NBUF 2
constexpr int S_QA_G1 = 0, S_QA_R = S_QA_G1 + NBUF;
constexpr int S_QA_G2 = 5, S_QA_L = S_QA_G2 + NBUF;
constexpr int SA = S_QA_L + 1;
constexpr int S_PA_C = SA + 2;
constexpr int S_PA_G = 12, S_PA_R = S_PA_G + NBUF;
constexpr int S_WO = 5, S_WOA = 13;
constexpr int S_YA = S_PA_R + 1;
constexpr unsigned SPIN_CAP = 1u << 18;

constexpr unsigned WS_GEN = 0;
constexpr unsigned WS_PQB = 4096;
constexpr unsigned WS_PQA = WS_PQB + 256 * 1024;
constexpr unsigned WS_QA = WS_PQA + 256 * 1024;
constexpr unsigned PA_REC = 1152;
constexpr unsigned WS_PA = WS_QA + 8 * 32 * 128;
constexpr unsigned WS_MLB = WS_PA + 8 * 32 * 4 * PA_REC;
constexpr unsigned WS_YA = WS_MLB + 256 * 128;
constexpr unsigned WS_YB = WS_YA + (1u << 20);
constexpr unsigned WS_END = WS_YB + (1u << 20);
#define ABL 0
#define PRETOUCH 1
#define PRO_MODE 2
#define WQ_AUX 2
#define WA_AUX 0
#define WO_AUX 2

constexpr int L_QPART = 0;
constexpr int L_QSUM = L_QPART + 2 * 8 * 128;
constexpr int L_PARTA = L_QSUM + 8 * 128;
constexpr int L_PARTB = L_PARTA + 8 * 4 * 132;
constexpr int L_MLB = L_PARTB + 8 * 128;
constexpr int L_YPART = L_MLB + 16;
constexpr int L_RED = L_YPART + 8 * 512;
constexpr int L_OFIN = L_RED + 16;
constexpr int L_QA = L_OFIN + 16;
constexpr int L_FLAG = L_QA + 256;
constexpr int L_END = L_FLAG + 4;

__device__ __forceinline__ rsrc_t mk_rsrc(const void* p, unsigned bytes) {
  return __builtin_amdgcn_make_buffer_rsrc(const_cast<void*>(p), 0, bytes, RSRC_W3);
}
template <int AUX> __device__ __forceinline__ v4u ld128(rsrc_t rs, unsigned voff, unsigned soff) {
  return __builtin_amdgcn_raw_buffer_load_b128(rs, voff, soff, AUX);
}
__device__ __forceinline__ f32x4 mfma(v4u a, v4u b, f32x4 c) {
  return __builtin_amdgcn_mfma_f32_16x16x32_bf16(__builtin_bit_cast(bf16x8, a), __builtin_bit_cast(bf16x8, b), c, 0, 0, 0);
}
__device__ __forceinline__ unsigned cvtpk(float lo, float hi) {
  f32x2 v = {lo, hi};
  return __builtin_bit_cast(unsigned, __builtin_convertvector(v, bf16x2_t));
}
template <int CTRL> __device__ __forceinline__ float dppf(float x) {
  return __builtin_bit_cast(float, __builtin_amdgcn_mov_dpp(__builtin_bit_cast(int, x), CTRL, 0xf, 0xf, true));
}
template <int CTRL, int RM, int BM> __device__ __forceinline__ unsigned dppu(unsigned old, unsigned x) {
  return (unsigned)__builtin_amdgcn_update_dpp((int)old, (int)x, CTRL, RM, BM, false);
}
constexpr int XOR1 = 0xB1, XOR2 = 0x4E, HMIRROR = 0x141, MIRROR = 0x140, ROR8 = 0x128, ROR4 = 0x124, BCAST = 0x150;
__device__ __forceinline__ float xrow_max(float x) {
  auto s = __builtin_amdgcn_permlane16_swap(__float_as_uint(x), __float_as_uint(x), false, false);
  x = fmaxf(__uint_as_float(s[0]), __uint_as_float(s[1]));
  auto t = __builtin_amdgcn_permlane32_swap(__float_as_uint(x), __float_as_uint(x), false, false);
  return fmaxf(__uint_as_float(t[0]), __uint_as_float(t[1]));
}
__device__ __forceinline__ float xrow_sum(float x) {
  auto s = __builtin_amdgcn_permlane16_swap(__float_as_uint(x), __float_as_uint(x), false, false);
  x = __uint_as_float(s[0]) + __uint_as_float(s[1]);
  auto t = __builtin_amdgcn_permlane32_swap(__float_as_uint(x), __float_as_uint(x), false, false);
  return __uint_as_float(t[0]) + __uint_as_float(t[1]);
}
__device__ __forceinline__ v4u andm(unsigned rep, const v4u& m) { return v4u{rep & m.x, rep & m.y, rep & m.z, rep & m.w}; }
__device__ __forceinline__ unsigned split_rep(float x, bool take_lo) {
  const unsigned hi = cvtpk(x, x);
  const float r = x - __uint_as_float(hi & 0xffff0000u);
  const unsigned lo = cvtpk(r, r);
  return take_lo ? lo : hi;
}
#define STAMP(k)
#define GIVEUP(code, okv)
#define SB() __builtin_amdgcn_sched_barrier(0)
#define MEMFENCE() asm volatile("" ::: "memory")
#define AWAIT(OK, RELOAD) do { if (__builtin_expect(!(OK), 0)) { unsigned spins_ = 0; \
    do { __builtin_amdgcn_s_sleep(2); MEMFENCE(); RELOAD; } while (!(OK) && ++spins_ < SPIN_CAP); } } while (0)
#define PIN(x) asm volatile("" : "+v"(x) :: "memory")

template <typename F, int... I>
__device__ __forceinline__ void run_slots(F& f, std::integer_sequence<int, I...>) { (f(std::integral_constant<int, I>{}), ...); }

struct State { float m, l; f32x4 acc; };

__device__ __forceinline__ void qk_part(const v4u (&K)[4], const v4u (&Bq)[4], f32x4& S) {
  S = f32x4{0.f, 0.f, 0.f, 0.f};
#pragma unroll
  for (int u = 0; u < 4; ++u) S = mfma(K[u], Bq[u], S);
}
__device__ __forceinline__ void sm_pv(const f32x4& S, const v4u (&V)[4], const v4u& mask, int n, State& st) {
  const float s01 = (n & 1) ? S[1] : S[0], s23 = (n & 1) ? S[3] : S[2];
  float s = (n & 2) ? s23 : s01;
  s += dppf<XOR1>(s);
  s += dppf<XOR2>(s);
  s = __uint_as_float(cvtpk(s, s) & 0xffff0000u) * SCALE;
  s = __uint_as_float(cvtpk(s, s) & 0xffff0000u);
  float tm = fmaxf(s, dppf<ROR4>(s));
  tm = fmaxf(tm, dppf<ROR8>(tm));
  tm = xrow_max(tm);
  const float mn = fmaxf(st.m, tm);
  const float alpha = __builtin_amdgcn_exp2f((st.m - mn) * C2);
  const float p = __builtin_amdgcn_exp2f((s - mn) * C2);
  st.m = mn;
  st.l = fmaf(st.l, alpha, p);
  st.acc *= alpha;
  const unsigned pk = split_rep(p, (n & 2) != 0);
  unsigned rep;
#define PV_STEP(u) rep = dppu<BCAST + 4 * (u), 0xf, 0x3>(pk, pk); rep = dppu<BCAST + 4 * (u) + 2, 0xf, 0xc>(rep, pk); \
  st.acc = mfma(V[u], andm(rep, mask), st.acc);
  PV_STEP(0) PV_STEP(1) PV_STEP(2) PV_STEP(3)
#undef PV_STEP
}
__device__ __forceinline__ void finish_state(State& st) {
  float l = st.l;
  l += dppf<ROR4>(l);
  l += dppf<ROR8>(l);
  st.l = xrow_sum(l);
#pragma unroll
  for (int i = 0; i < 4; ++i) st.acc[i] += dppf<ROR8>(st.acc[i]);
}
__device__ __forceinline__ int opos(int d) { return (d >> 4) * 16 + (d & 3) * 4 + ((d >> 2) & 3); }
__device__ __forceinline__ bool tag2(const v4u& x, unsigned tag) { return x.y == tag && x.w == tag; }

__global__ void __launch_bounds__(512, 2)
fused_decode(const uint16_t* __restrict__ xA, const uint16_t* __restrict__ kA, const uint16_t* __restrict__ vA,
             const uint16_t* __restrict__ wqA, const uint16_t* __restrict__ woA,
             const uint16_t* __restrict__ xB, const uint16_t* __restrict__ kB, const uint16_t* __restrict__ vB,
             const uint16_t* __restrict__ wqB, const uint16_t* __restrict__ woB,
             uint16_t* __restrict__ out, unsigned char* ws) {
  __shared__ __attribute__((aligned(16))) float lds[L_END];
  const int tid = threadIdx.x, lane = tid & 63, wv = __builtin_amdgcn_readfirstlane(tid >> 6);
  const int g = lane >> 4, n = lane & 15;
  STAMP(0);
  const int w = blockIdx.x, x8 = w & 7, j = w >> 3;
  const int hB = 4 * x8 + (j & 3), sB = j >> 2;

  const rsrc_t rKB = mk_rsrc(kB + ((size_t)hB * SK + sB * 2048) * 128, 2048 * 256);
  const rsrc_t rVB = mk_rsrc(vB + ((size_t)hB * SK + sB * 2048) * 128, 2048 * 256);
  const rsrc_t rKA = mk_rsrc(kA + ((size_t)x8 * SK + WIN0 + j * 128) * 128, 128 * 256);
  const rsrc_t rVA = mk_rsrc(vA + ((size_t)x8 * SK + WIN0 + j * 128) * 128, 128 * 256);
  const rsrc_t rWQB = mk_rsrc(wqB, 512 * 8192);
  const rsrc_t rWQA = mk_rsrc(wqA, 512 * 8192);
  const rsrc_t rWOB = mk_rsrc(woB, 4096 * 1024);
  const rsrc_t rWOA = mk_rsrc(woA, 4096 * 1024);
  const rsrc_t rXB = mk_rsrc(xB, 1024);
  const rsrc_t rXA = mk_rsrc(xA, 1024);
  const rsrc_t rWS = mk_rsrc(ws, WS_END);

  const unsigned koff = (n >> 2) * 256 + g * 64 + (n & 3) * 16, voff = lane * 16;
  const unsigned wvoff = wv * 4096;

  v4u qa, g1, g2[8], woa[2], wob[16];
  v4u wqb[2], wqa[2];
  unsigned short xbr[2], xar[2];
  const int hAq = 4 * x8 + (j >> 3), ks = j & 7;
#pragma unroll
  for (int t = 0; t < 2; ++t) {
    wqb[t] = ld128<WQ_AUX>(rWQB, g * 8192 + n * 16, (64 * sB + 8 * wv + 4 * t) * 8192 + hB * 256);
    xbr[t] = __builtin_amdgcn_raw_buffer_load_b16(rXB, g * 2, (64 * sB + 8 * wv + 4 * t) * 2, 0);
  }
#pragma unroll
  for (int t = 0; t < 2; ++t) {
    wqa[t] = ld128<WQ_AUX>(rWQA, g * 8192 + n * 16, (64 * ks + 8 * wv + 4 * t) * 8192 + hAq * 256);
    xar[t] = __builtin_amdgcn_raw_buffer_load_b16(rXA, g * 2, (64 * ks + 8 * wv + 4 * t) * 2, 0);
  }
  const unsigned gen = __builtin_amdgcn_raw_buffer_load_b32(rWS, WS_GEN, 0, AUX_SC1);
  unsigned pt[6];
  pt[0] = __builtin_amdgcn_raw_buffer_load_b32(rKA, 0, wvoff, AUX_NT);
  pt[1] = __builtin_amdgcn_raw_buffer_load_b32(rVA, 0, wvoff, AUX_NT);
  pt[2] = __builtin_amdgcn_raw_buffer_load_b32(rWOA, 0, (512 * x8 + 16 * j) * 1024, AUX_NT);
  pt[3] = __builtin_amdgcn_raw_buffer_load_b32(rWOB, 0, (hB * 128 + 16 * wv) * 1024, AUX_NT);
  pt[4] = __builtin_amdgcn_raw_buffer_load_b32(rWS, 0, WS_YA + w * 4096, AUX_SC1);
  pt[5] = __builtin_amdgcn_raw_buffer_load_b32(rWS, 0, WS_YB + w * 4096, AUX_SC1);
  SB();
  v4u K[NSLOT][4], V[NSLOT][4];
  auto issue = [&](int u) __attribute__((always_inline)) {
    const int s = u >> 1;
    const bool isA = s == SA;
    const int tb = s < SA ? s : s - 1;
    if (u & 1) {
#pragma unroll
      for (int k = 0; k < 4; ++k) V[s][k] = ld128<AUX_NT>(isA ? rVA : rVB, voff + k * 1024, wvoff + (isA ? 0 : tb * 32768));
    } else {
#pragma unroll
      for (int k = 0; k < 4; ++k) K[s][k] = ld128<AUX_NT>(isA ? rKA : rKB, koff + k * 1024, wvoff + (isA ? 0 : tb * 32768));
    }
  };
  if (tid < 4) lds[L_FLAG + tid] = 0.f;
  __builtin_amdgcn_s_barrier();
  issue(0);
  SB();
  const unsigned tag = __builtin_amdgcn_readfirstlane(gen) + 1u;
  v4u mask;
  {
    const unsigned hm = (n & 1) ? 0xffff0000u : 0x0000ffffu;
    const int q = (n & 7) >> 1;
    mask = v4u{q == 0 ? hm : 0u, q == 1 ? hm : 0u, q == 2 ? hm : 0u, q == 3 ? hm : 0u};
  }
  {
    f32x4 DB = {0.f, 0.f, 0.f, 0.f}, DA = {0.f, 0.f, 0.f, 0.f};
#pragma unroll
    for (int t = 0; t < 2; ++t) {
      DB = mfma(wqb[t], andm((unsigned)xbr[t] * 0x10001u, mask), DB);
      DA = mfma(wqa[t], andm((unsigned)xar[t] * 0x10001u, mask), DA);
    }
    if (n < 8) {
      *(f32x4*)&lds[L_QPART + wv * 128 + (g * 8 + n) * 4] = DB;
      *(f32x4*)&lds[L_QPART + 1024 + wv * 128 + (g * 8 + n) * 4] = DA;
    }
  }
  STAMP(24);
  __syncthreads();
  if (wv < 2) {
    const int c = 2 * lane;
    const int e = ((c >> 5) * 8 + (c & 7)) * 4 + ((c >> 3) & 3);
    float y0 = 0.f, y1 = 0.f;
#pragma unroll
    for (int k = 0; k < 8; ++k) { y0 += lds[L_QPART + wv * 1024 + k * 128 + e]; y1 += lds[L_QPART + wv * 1024 + k * 128 + e + 4]; }
    const unsigned dst = wv == 0 ? WS_PQB + (hB * 8 + sB) * 1024 : WS_PQA + (hAq * 8 + ks) * 1024;
    __builtin_amdgcn_raw_buffer_store_b128(v4u{__float_as_uint(y0), tag, __float_as_uint(y1), tag}, rWS, dst + lane * 16, 0, AUX_SC1);
  }
  STAMP(25);
  SB();
  issue(1);
  SB();
  PIN(K[0][3]);
  issue(2);
  SB();
  PIN(V[0][3]);
  STAMP(26);
  {
    const unsigned src = WS_PQB + (hB * 8 + wv) * 1024 + lane * 16;
    v4u pq = ld128<AUX_SC1>(rWS, src, 0);
    SB();
    issue(3);
    SB();
    PIN(K[1][3]);
    STAMP(28);
    issue(4);
    SB();
    AWAIT(__all(tag2(pq, tag)), pq = ld128<AUX_SC1>(rWS, src, 0));
    *(f32x2*)&lds[L_QSUM + wv * 128 + 2 * lane] = f32x2{__uint_as_float(pq.x), __uint_as_float(pq.z)};
  }
  asm volatile("" :: "v"(pt[0]), "v"(pt[1]), "v"(pt[2]), "v"(pt[3]), "v"(pt[4]), "v"(pt[5]));
  STAMP(27);
  __syncthreads();
  v4u qB;
  {
    f32x4 a = {0.f, 0.f, 0.f, 0.f}, b = {0.f, 0.f, 0.f, 0.f};
#pragma unroll
    for (int k = 0; k < 8; ++k) {
      a += *(const f32x4*)&lds[L_QSUM + k * 128 + 8 * (4 * g + (n & 3))];
      b += *(const f32x4*)&lds[L_QSUM + k * 128 + 8 * (4 * g + (n & 3)) + 4];
    }
    qB = v4u{cvtpk(a[0], a[1]), cvtpk(a[2], a[3]), cvtpk(b[0], b[1]), cvtpk(b[2], b[3])};
  }
  v4u Bq[4];
  auto make_bq = [&](const v4u& q) __attribute__((always_inline)) {
#pragma unroll
    for (int u = 0; u < 4; ++u) Bq[u] = ((n >> 2) == u) ? q : v4u{0u, 0u, 0u, 0u};
  };
  make_bq(qB);
  STAMP(1);
  State st;
  st.m = -INFINITY; st.l = 0.f; st.acc = f32x4{0.f, 0.f, 0.f, 0.f};

  auto slot = [&](auto S_) __attribute__((always_inline)) {
    constexpr int s = decltype(S_)::value;
    if constexpr (s != SA || (ABL & 1)) {
      f32x4 S;
      qk_part(K[s], Bq, S);
      PIN(S);
      SB();
      if constexpr (next_unit(2 * s) >= 0) issue(next_unit(2 * s));
      SB();
      sm_pv(S, V[s], mask, n, st);
      PIN(st.acc); PIN(st.l);
      SB();
      if constexpr (next_unit(2 * s + 1) >= 0) issue(next_unit(2 * s + 1));
      SB();
    } else {
      issue(next_unit(2 * s)); issue(next_unit(2 * s + 1));
      SB();
      while (__hip_atomic_load((unsigned*)&lds[L_FLAG + 2], __ATOMIC_RELAXED, __HIP_MEMORY_SCOPE_WORKGROUP) != 2u) __builtin_amdgcn_s_sleep(1);
      MEMFENCE();
      State sa[4];
#pragma unroll
      for (int hd = 0; hd < 4; ++hd) {
        const v4u q = *(const v4u*)&lds[L_QA + hd * 64 + (4 * g + (n & 3)) * 4];
        v4u Ba[4];
#pragma unroll
        for (int u = 0; u < 4; ++u) Ba[u] = ((n >> 2) == u) ? q : v4u{0u, 0u, 0u, 0u};
        sa[hd].m = -INFINITY; sa[hd].l = 0.f; sa[hd].acc = f32x4{0.f, 0.f, 0.f, 0.f};
        f32x4 S;
        qk_part(K[s], Ba, S);
        sm_pv(S, V[s], mask, n, sa[hd]);
        finish_state(sa[hd]);
      }
#pragma unroll
      for (int hd = 0; hd < 4; ++hd) {
        float* rec = &lds[L_PARTA + (wv * 4 + hd) * 132];
        if (n < 8) {
#pragma unroll
          for (int i = 0; i < 4; ++i) rec[4 + 32 * g + 8 * i + n] = sa[hd].acc[i];
        }
        if (lane == 0) { rec[0] = sa[hd].m; rec[1] = sa[hd].l; }
      }
      asm volatile("s_waitcnt lgkmcnt(0)" ::: "memory");
      if (lane == 0) __hip_atomic_fetch_add((unsigned*)&lds[L_FLAG], 1u, __ATOMIC_RELAXED, __HIP_MEMORY_SCOPE_WORKGROUP);
      SB();
    }
    if constexpr (s == S_PA_C && !(ABL & 1)) {
      while (__hip_atomic_load((unsigned*)&lds[L_FLAG], __ATOMIC_RELAXED, __HIP_MEMORY_SCOPE_WORKGROUP) != 8u) __builtin_amdgcn_s_sleep(1);
      MEMFENCE();
      const int hd = wv >> 1, d = 64 * (wv & 1) + lane;
      float M = -INFINITY;
#pragma unroll
      for (int k = 0; k < 8; ++k) M = fmaxf(M, lds[L_PARTA + (k * 4 + hd) * 132]);
      float o = 0.f, L = 0.f;
#pragma unroll
      for (int k = 0; k < 8; ++k) {
        const float* rec = &lds[L_PARTA + (k * 4 + hd) * 132];
        const float f = __builtin_amdgcn_exp2f((rec[0] - M) * C2);
        o = fmaf(f, rec[4 + d], o);
        L = fmaf(f, rec[1], L);
      }
      const unsigned base = WS_PA + ((x8 * 32 + j) * 4 + hd) * PA_REC;
      __builtin_amdgcn_raw_buffer_store_b64(v2u{__float_as_uint(o), tag}, rWS, base + 16 + d * 8, 0, AUX_SC1);
      if (d == 0) __builtin_amdgcn_raw_buffer_store_b128(v4u{__float_as_uint(M), tag, __float_as_uint(L), tag}, rWS, base, 0, AUX_SC1);
      SB();
    }
    STAMP(2 + s);
    if constexpr (s % LOCKSTEP == LOCKSTEP - 1 && s + 1 < NSLOT) { SB(); __builtin_amdgcn_s_barrier(); SB(); }
    if constexpr (s == S_QA_G1 && !(ABL & 1)) if (wv == 4) {
      g1 = ld128<AUX_SC1>(rWS, WS_PQA + (hAq * 8 + (lane >> 3)) * 1024 + (16 * ks + 2 * (lane & 7)) * 8, 0);
      SB();
    }
    if constexpr (s == S_QA_R && !(ABL & 1)) if (wv == 4) {
      AWAIT(__all(tag2(g1, tag)), g1 = ld128<AUX_SC1>(rWS, WS_PQA + (hAq * 8 + (lane >> 3)) * 1024 + (16 * ks + 2 * (lane & 7)) * 8, 0));
      float v0 = __uint_as_float(g1.x), v1 = __uint_as_float(g1.z);
      v0 += dppf<ROR8>(v0); v1 += dppf<ROR8>(v1);
      v0 = xrow_sum(v0); v1 = xrow_sum(v1);
      if (lane < 8) __builtin_amdgcn_raw_buffer_store_b64(v2u{cvtpk(v0, v1), tag}, rWS, WS_QA + (x8 * 32 + j) * 128 + lane * 8, 0, AUX_SC1);
      SB();
    }
    if constexpr (s == S_QA_G2 && !(ABL & 1)) if (wv < 2) {
      qa = ld128<AUX_SC1>(rWS, WS_QA + (x8 * 32 + 16 * wv + (lane >> 2)) * 128 + (lane & 3) * 16, 0);
      SB();
    }
    if constexpr (s == S_QA_L && !(ABL & 1)) if (wv < 2) {
      AWAIT(__all(tag2(qa, tag)), qa = ld128<AUX_SC1>(rWS, WS_QA + (x8 * 32 + 16 * wv + (lane >> 2)) * 128 + (lane & 3) * 16, 0));
      *(v2u*)&lds[L_QA + 2 * (64 * wv + lane)] = v2u{qa.x, qa.z};
      asm volatile("s_waitcnt lgkmcnt(0)" ::: "memory");
      if (lane == 0) __hip_atomic_fetch_add((unsigned*)&lds[L_FLAG + 2], 1u, __ATOMIC_RELAXED, __HIP_MEMORY_SCOPE_WORKGROUP);
      SB();
    }
    if constexpr (s == S_PA_G && !(ABL & 1)) if (wv == 7) {
      const unsigned base = WS_PA + ((x8 * 32 + (lane >> 3)) * 4 + (j >> 3)) * PA_REC;
#pragma unroll
      for (int k = 0; k < 4; ++k) {
        g2[k] = ld128<AUX_SC1>(rWS, base + k * 8 * 4 * PA_REC, 0);
        g2[4 + k] = ld128<AUX_SC1>(rWS, base + k * 8 * 4 * PA_REC + 16 + (16 * (j & 7) + 2 * (lane & 7)) * 8, 0);
      }
      SB();
    }
    if constexpr (s == S_PA_R && !(ABL & 1)) if (wv == 7) {
      const unsigned base = WS_PA + ((x8 * 32 + (lane >> 3)) * 4 + (j >> 3)) * PA_REC;
      auto g2_load = [&]() __attribute__((always_inline)) {
#pragma unroll
        for (int k = 0; k < 4; ++k) {
          g2[k] = ld128<AUX_SC1>(rWS, base + k * 8 * 4 * PA_REC, 0);
          g2[4 + k] = ld128<AUX_SC1>(rWS, base + k * 8 * 4 * PA_REC + 16 + (16 * (j & 7) + 2 * (lane & 7)) * 8, 0);
        }
      };
      AWAIT(__all(tag2(g2[0], tag) && tag2(g2[1], tag) && tag2(g2[2], tag) && tag2(g2[3], tag) && tag2(g2[4], tag) && tag2(g2[5], tag) &&
                  tag2(g2[6], tag) && tag2(g2[7], tag)), g2_load());
      float M = -INFINITY;
#pragma unroll
      for (int k = 0; k < 4; ++k) M = fmaxf(M, __uint_as_float(g2[k].x));
      M = fmaxf(M, dppf<ROR8>(M));
      M = xrow_max(M);
      float L = 0.f, a0 = 0.f, a1 = 0.f;
#pragma unroll
      for (int k = 0; k < 4; ++k) {
        const float f = __builtin_amdgcn_exp2f((__uint_as_float(g2[k].x) - M) * C2);
        L = fmaf(f, __uint_as_float(g2[k].z), L);
        a0 = fmaf(f, __uint_as_float(g2[4 + k].x), a0);
        a1 = fmaf(f, __uint_as_float(g2[4 + k].z), a1);
      }
      L += dppf<ROR8>(L); a0 += dppf<ROR8>(a0); a1 += dppf<ROR8>(a1);
      L = xrow_sum(L); a0 = xrow_sum(a0); a1 = xrow_sum(a1);
      if (lane < 8) *(f32x2*)&lds[L_OFIN + 2 * lane] = f32x2{a0 / L, a1 / L};
      asm volatile("s_waitcnt lgkmcnt(0)" ::: "memory");
      if (lane == 0) __hip_atomic_store((unsigned*)&lds[L_FLAG + 1], 1u, __ATOMIC_RELAXED, __HIP_MEMORY_SCOPE_WORKGROUP);
      SB();
    }
    if constexpr (s == S_YA && !(ABL & 1)) {
      while (__hip_atomic_load((unsigned*)&lds[L_FLAG + 1], __ATOMIC_RELAXED, __HIP_MEMORY_SCOPE_WORKGROUP) != 1u) __builtin_amdgcn_s_sleep(1);
      MEMFENCE();
      f32x4 Y = {0.f, 0.f, 0.f, 0.f};
#pragma unroll
      for (int t = 0; t < 2; ++t) {
        const float o = lds[L_OFIN + 8 * t + 2 * g + (n >> 3)];
        Y = mfma(woa[t], andm(split_rep(o, false), mask), Y);
        Y = mfma(woa[t], andm(split_rep(o, true), mask), Y);
      }
      const bool valid = (g >> 1) == (n >> 3);
      float z[4];
#pragma unroll
      for (int i = 0; i < 4; ++i) {
        float t = valid ? Y[i] : 0.f;
        t += dppf<ROR8>(t);
        auto sw = __builtin_amdgcn_permlane32_swap(__float_as_uint(t), __float_as_uint(t), false, false);
        z[i] = __uint_as_float(sw[0]) + __uint_as_float(sw[1]);
      }
      const int i = 2 * (g >> 1) + (n >> 3);
      const float zi = (i & 2) ? ((i & 1) ? z[3] : z[2]) : ((i & 1) ? z[1] : z[0]);
      __builtin_amdgcn_raw_buffer_store_b64(v2u{__float_as_uint(zi), tag}, rWS,
                                            WS_YA + w * 4096 + (64 * wv + 32 * (g & 1) + 8 * i + (n & 7)) * 8, 0, AUX_SC1);
      SB();
    }
    if constexpr (s >= S_WO && s < S_WO + 8 && !(ABL & 2)) {
#pragma unroll
      for (int k = 2 * (s - S_WO); k < 2 * (s - S_WO) + 2; ++k)
        wob[k] = ld128<WO_AUX>(rWOB, g * 1024 + n * 16, (hB * 128 + 16 * wv + 4 * (k >> 2)) * 1024 + (k & 3) * 256);
      SB();
    }
    if constexpr (s == S_WOA && !(ABL & 1)) {
#pragma unroll
      for (int t = 0; t < 2; ++t)
        woa[t] = ld128<AUX_NT>(rWOA, (lane >> 3) * 1024 + (lane & 7) * 16, (512 * x8 + 16 * j + 8 * t) * 1024 + wv * 128);
      SB();
    }
  };
  run_slots(slot, std::make_integer_sequence<int, NSLOT>{});

  STAMP(19);
  finish_state(st);
  if (n < 8) {
#pragma unroll
    for (int i = 0; i < 4; ++i) lds[L_PARTB + wv * 128 + opos(32 * g + 8 * i + n)] = st.acc[i];
  }
  if (lane == 0) { lds[L_MLB + 2 * wv] = st.m; lds[L_MLB + 2 * wv + 1] = st.l; }
  __syncthreads();
  {
    float M = -INFINITY;
#pragma unroll
    for (int k = 0; k < 8; ++k) M = fmaxf(M, lds[L_MLB + 2 * k]);
    float L = 0.f;
    f32x4 o4 = {0.f, 0.f, 0.f, 0.f};
#pragma unroll
    for (int k = 0; k < 8; ++k) {
      const float f = __builtin_amdgcn_exp2f((lds[L_MLB + 2 * k] - M) * C2);
      L = fmaf(f, lds[L_MLB + 2 * k + 1], L);
      o4 += f * *(const f32x4*)&lds[L_PARTB + k * 128 + 16 * wv + 4 * g];
    }
    if (tid == 0)
      __builtin_amdgcn_raw_buffer_store_b128(v4u{__float_as_uint(M), tag, __float_as_uint(L), tag}, rWS, WS_MLB + (hB * 8 + sB) * 128, 0, AUX_SC1);
    f32x4 Y[4];
#pragma unroll
    for (int cb = 0; cb < 4; ++cb) Y[cb] = f32x4{0.f, 0.f, 0.f, 0.f};
#pragma unroll
    for (int rg = 0; rg < 4; ++rg) {
      const v4u fr = andm(split_rep(o4[rg], n >= 8), mask);
#pragma unroll
      for (int cb = 0; cb < 4; ++cb) Y[cb] = mfma(wob[rg * 4 + cb], fr, Y[cb]);
    }
#pragma unroll
    for (int cb = 0; cb < 4; ++cb) {
#pragma unroll
      for (int i = 0; i < 4; ++i) Y[cb][i] += dppf<ROR8>(Y[cb][i]);
      if (n < 8) *(f32x4*)&lds[L_YPART + wv * 512 + ((cb * 4 + g) * 8 + n) * 4] = Y[cb];
    }
  }
  __syncthreads();
  STAMP(20);
  if (tid < 256) {
    const int c = 2 * tid;
    const int idx = (((c >> 7) * 4 + ((c >> 5) & 3)) * 8 + (c & 7)) * 4 + ((c >> 3) & 3);
    float y0 = 0.f, y1 = 0.f;
#pragma unroll
    for (int k = 0; k < 8; ++k) { y0 += lds[L_YPART + k * 512 + idx]; y1 += lds[L_YPART + k * 512 + idx + 4]; }
    __builtin_amdgcn_raw_buffer_store_b128(v4u{__float_as_uint(y0), tag, __float_as_uint(y1), tag}, rWS,
                                           WS_YB + (hB * 8 + sB) * 4096 + tid * 16, 0, AUX_SC1);
  }
  STAMP(21);
  {
    const bool isB = wv < 4;
    const unsigned yoff = (isB ? WS_YB : WS_YA) + (tid & 255) * 4096 + w * 16;
    v4u y, ml = v4u{0u, tag, 0u, tag};
    for (unsigned spins = 0;; ++spins) {
      MEMFENCE();
      y = ld128<AUX_SC1>(rWS, yoff, 0);
      if (isB) ml = ld128<AUX_SC1>(rWS, WS_MLB + tid * 128, 0);
#define DBGV (tag - y.y)
      GIVEUP(3, tag2(y, tag));
      GIVEUP(4, tag2(ml, tag));
#undef DBGV
      if (__all(tag2(y, tag) && tag2(ml, tag)) || spins > SPIN_CAP || ((ABL & 1) && !isB)) break;
      __builtin_amdgcn_s_sleep(1);
    }
    STAMP(22);
    float v0 = __uint_as_float(y.x), v1 = __uint_as_float(y.z);
    if (isB) {
      const float m = __uint_as_float(ml.x);
      float M = fmaxf(m, dppf<XOR1>(m));
      M = fmaxf(M, dppf<XOR2>(M));
      M = fmaxf(M, dppf<HMIRROR>(M));
      const float f = __builtin_amdgcn_exp2f((m - M) * C2);
      float L = f * __uint_as_float(ml.z);
      L += dppf<XOR1>(L); L += dppf<XOR2>(L); L += dppf<HMIRROR>(L);
      const float c = f / L;
      v0 *= c; v1 *= c;
    }
    v0 += dppf<XOR1>(v0); v0 += dppf<XOR2>(v0); v0 += dppf<ROR4>(v0); v0 += dppf<ROR8>(v0); v0 = xrow_sum(v0);
    v1 += dppf<XOR1>(v1); v1 += dppf<XOR2>(v1); v1 += dppf<ROR4>(v1); v1 += dppf<ROR8>(v1); v1 = xrow_sum(v1);
    if (lane == 0) { lds[L_RED + 2 * wv] = v0; lds[L_RED + 2 * wv + 1] = v1; }
  }
  __syncthreads();
  if (lane == 0 && (wv == 0 || wv == 4)) {
    const float* r = &lds[L_RED + 2 * wv];
    const float s0 = (r[0] + r[2]) + (r[4] + r[6]), s1 = (r[1] + r[3]) + (r[5] + r[7]);
    *(unsigned*)(out + (wv == 0 ? 512 : 0) + 2 * w) = cvtpk(s0, s1);
    if (w == 0 && wv == 0) __builtin_amdgcn_raw_buffer_store_b32(tag, rWS, WS_GEN, 0, AUX_SC1);
  }
}

}

extern "C" void kernel_launch(void* const* d_in, const int* in_sizes, int n_in,
                              void* d_out, int out_size, void* d_ws, size_t ws_size,
                              hipStream_t stream) {
  (void)in_sizes; (void)n_in; (void)out_size; (void)ws_size;
  fused_decode<<<256, 512, 0, stream>>>(
      (const uint16_t*)d_in[0], (const uint16_t*)d_in[1], (const uint16_t*)d_in[2], (const uint16_t*)d_in[3], (const uint16_t*)d_in[4],
      (const uint16_t*)d_in[5], (const uint16_t*)d_in[6], (const uint16_t*)d_in[7], (const uint16_t*)d_in[8], (const uint16_t*)d_in[9],
      (uint16_t*)d_out, (unsigned char*)d_ws);
}
```

```cpp
#define V_AUX 0
#define WQ_AUX 0
#define WO_AUX 0
#include <hip/hip_runtime.h>
#include <hip/hip_bf16.h>
#include <stdint.h>
#include <utility>

#define GAS __attribute__((address_space(1)))
typedef unsigned v4u __attribute__((ext_vector_type(4)));
typedef unsigned v2u __attribute__((ext_vector_type(2)));
typedef float f32x4 __attribute__((ext_vector_type(4)));
typedef float f32x2 __attribute__((ext_vector_type(2)));
typedef short bf16x8 __attribute__((ext_vector_type(8)));
typedef __bf16 bf16x2_t __attribute__((ext_vector_type(2)));
typedef __amdgpu_buffer_rsrc_t rsrc_t;

namespace {

constexpr int AUX_NT = 2, AUX_SC1 = 16;
constexpr int RSRC_W3 = 0x00020000;
constexpr float SCALE = 0.08838834764831845f;
constexpr float C2 = 1.4426950408889634f;
constexpr int SK = 16384, WIN0 = 16384 - 4096;
constexpr int NSLOT = 17;
#define DIST 4
constexpr int PRE = 5;
constexpr int next_unit(int u) { return (u + DIST >= PRE && u + DIST < 2 * NSLOT) ? u + DIST : -1; }
#define NBUF 2
constexpr int S_QA_G1 = 0, S_QA_R = S_QA_G1 + NBUF;
constexpr int S_QA_G2 = 5, S_QA_L = S_QA_G2 + NBUF;
constexpr int SA = S_QA_L + 1;
constexpr int S_PA_C = SA + 2;
constexpr int S_PA_G = 12, S_PA_R = S_PA_G + NBUF;
constexpr int S_WO = 5, S_WOA = 13;
constexpr int S_YA = S_PA_R + 1;
constexpr unsigned SPIN_CAP = 1u << 18;

constexpr unsigned WS_GEN = 0;
constexpr unsigned WS_PQB = 4096;
constexpr unsigned WS_PQA = WS_PQB + 256 * 1024;
constexpr unsigned WS_QA = WS_PQA + 256 * 1024;
constexpr unsigned PA_REC = 1152;
constexpr unsigned WS_PA = WS_QA + 8 * 32 * 128;
constexpr unsigned WS_MLB = WS_PA + 8 * 32 * 4 * PA_REC;
constexpr unsigned WS_YA = WS_MLB + 256 * 128;
constexpr unsigned WS_YB = WS_YA + (1u << 20);
constexpr unsigned WS_END = WS_YB + (1u << 20);
#define ABL 0
#define PRETOUCH 1
#define LOCKSTEP 2
#define PRO_MODE 2
#define K_AUX 2
#define WA_AUX 0

constexpr int L_QPART = 0;
constexpr int L_QSUM = L_QPART + 2 * 8 * 128;
constexpr int L_PARTA = L_QSUM + 8 * 128;
constexpr int L_PARTB = L_PARTA + 8 * 4 * 132;
constexpr int L_MLB = L_PARTB + 8 * 128;
constexpr int L_YPART = L_MLB + 16;
constexpr int L_RED = L_YPART + 8 * 512;
constexpr int L_OFIN = L_RED + 16;
constexpr int L_QA = L_OFIN + 16;
constexpr int L_FLAG = L_QA + 256;
constexpr int L_END = L_FLAG + 4;

__device__ __forceinline__ rsrc_t mk_rsrc(const void* p, unsigned bytes) {
  return __builtin_amdgcn_make_buffer_rsrc(const_cast<void*>(p), 0, bytes, RSRC_W3);
}
template <int AUX> __device__ __forceinline__ v4u ld128(rsrc_t rs, unsigned voff, unsigned soff) {
  return __builtin_amdgcn_raw_buffer_load_b128(rs, voff, soff, AUX);
}
__device__ __forceinline__ f32x4 mfma(v4u a, v4u b, f32x4 c) {
  return __builtin_amdgcn_mfma_f32_16x16x32_bf16(__builtin_bit_cast(bf16x8, a), __builtin_bit_cast(bf16x8, b), c, 0, 0, 0);
}
__device__ __forceinline__ unsigned cvtpk(float lo, float hi) {
  f32x2 v = {lo, hi};
  return __builtin_bit_cast(unsigned, __builtin_convertvector(v, bf16x2_t));
}
template <int CTRL> __device__ __forceinline__ float dppf(float x) {
  return __builtin_bit_cast(float, __builtin_amdgcn_mov_dpp(__builtin_bit_cast(int, x), CTRL, 0xf, 0xf, true));
}
template <int CTRL, int RM, int BM> __device__ __forceinline__ unsigned dppu(unsigned old, unsigned x) {
  return (unsigned)__builtin_amdgcn_update_dpp((int)old, (int)x, CTRL, RM, BM, false);
}
constexpr int XOR1 = 0xB1, XOR2 = 0x4E, HMIRROR = 0x141, MIRROR = 0x140, ROR8 = 0x128, ROR4 = 0x124, BCAST = 0x150;
__device__ __forceinline__ float xrow_max(float x) {
  auto s = __builtin_amdgcn_permlane16_swap(__float_as_uint(x), __float_as_uint(x), false, false);
  x = fmaxf(__uint_as_float(s[0]), __uint_as_float(s[1]));
  auto t = __builtin_amdgcn_permlane32_swap(__float_as_uint(x), __float_as_uint(x), false, false);
  return fmaxf(__uint_as_float(t[0]), __uint_as_float(t[1]));
}
__device__ __forceinline__ float xrow_sum(float x) {
  auto s = __builtin_amdgcn_permlane16_swap(__float_as_uint(x), __float_as_uint(x), false, false);
  x = __uint_as_float(s[0]) + __uint_as_float(s[1]);
  auto t = __builtin_amdgcn_permlane32_swap(__float_as_uint(x), __float_as_uint(x), false, false);
  return __uint_as_float(t[0]) + __uint_as_float(t[1]);
}
__device__ __forceinline__ v4u andm(unsigned rep, const v4u& m) { return v4u{rep & m.x, rep & m.y, rep & m.z, rep & m.w}; }
__device__ __forceinline__ unsigned split_rep(float x, bool take_lo) {
  const unsigned hi = cvtpk(x, x);
  const float r = x - __uint_as_float(hi & 0xffff0000u);
  const unsigned lo = cvtpk(r, r);
  return take_lo ? lo : hi;
}
#define STAMP(k)
#define GIVEUP(code, okv)
#define SB() __builtin_amdgcn_sched_barrier(0)
#define MEMFENCE() asm volatile("" ::: "memory")
#define AWAIT(OK, RELOAD) do { if (__builtin_expect(!(OK), 0)) { unsigned spins_ = 0; \
    do { __builtin_amdgcn_s_sleep(2); MEMFENCE(); RELOAD; } while (!(OK) && ++spins_ < SPIN_CAP); } } while (0)
#define PIN(x) asm volatile("" : "+v"(x) :: "memory")

template <typename F, int... I>
__device__ __forceinline__ void run_slots(F& f, std::integer_sequence<int, I...>) { (f(std::integral_constant<int, I>{}), ...); }

struct State { float m, l; f32x4 acc; };

__device__ __forceinline__ void qk_part(const v4u (&K)[4], const v4u (&Bq)[4], f32x4& S) {
  S = f32x4{0.f, 0.f, 0.f, 0.f};
#pragma unroll
  for (int u = 0; u < 4; ++u) S = mfma(K[u], Bq[u], S);
}
__device__ __forceinline__ void sm_pv(const f32x4& S, const v4u (&V)[4], const v4u& mask, int n, State& st) {
  const float s01 = (n & 1) ? S[1] : S[0], s23 = (n & 1) ? S[3] : S[2];
  float s = (n & 2) ? s23 : s01;
  s += dppf<XOR1>(s);
  s += dppf<XOR2>(s);
  s = __uint_as_float(cvtpk(s, s) & 0xffff0000u) * SCALE;
  s = __uint_as_float(cvtpk(s, s) & 0xffff0000u);
  float tm = fmaxf(s, dppf<ROR4>(s));
  tm = fmaxf(tm, dppf<ROR8>(tm));
  tm = xrow_max(tm);
  const float mn = fmaxf(st.m, tm);
  const float alpha = __builtin_amdgcn_exp2f((st.m - mn) * C2);
  const float p = __builtin_amdgcn_exp2f((s - mn) * C2);
  st.m = mn;
  st.l = fmaf(st.l, alpha, p);
  st.acc *= alpha;
  const unsigned pk = split_rep(p, (n & 2) != 0);
  unsigned rep;
#define PV_STEP(u) rep = dppu<BCAST + 4 * (u), 0xf, 0x3>(pk, pk); rep = dppu<BCAST + 4 * (u) + 2, 0xf, 0xc>(rep, pk); \
  st.acc = mfma(V[u], andm(rep, mask), st.acc);
  PV_STEP(0) PV_STEP(1) PV_STEP(2) PV_STEP(3)
#undef PV_STEP
}
__device__ __forceinline__ void finish_state(State& st) {
  float l = st.l;
  l += dppf<ROR4>(l);
  l += dppf<ROR8>(l);
  st.l = xrow_sum(l);
#pragma unroll
  for (int i = 0; i < 4; ++i) st.acc[i] += dppf<ROR8>(st.acc[i]);
}
__device__ __forceinline__ int opos(int d) { return (d >> 4) * 16 + (d & 3) * 4 + ((d >> 2) & 3); }
__device__ __forceinline__ bool tag2(const v4u& x, unsigned tag) { return x.y == tag && x.w == tag; }

__global__ void __launch_bounds__(512, 2)
fused_decode(const uint16_t* __restrict__ xA, const uint16_t* __restrict__ kA, const uint16_t* __restrict__ vA,
             const uint16_t* __restrict__ wqA, const uint16_t* __restrict__ woA,
             const uint16_t* __restrict__ xB, const uint16_t* __restrict__ kB, const uint16_t* __restrict__ vB,
             const uint16_t* __restrict__ wqB, const uint16_t* __restrict__ woB,
             uint16_t* __restrict__ out, unsigned char* ws) {
  __shared__ __attribute__((aligned(16))) float lds[L_END];
  const int tid = threadIdx.x, lane = tid & 63, wv = __builtin_amdgcn_readfirstlane(tid >> 6);
  const int g = lane >> 4, n = lane & 15;
  STAMP(0);
  const int w = blockIdx.x, x8 = w & 7, j = w >> 3;
  const int hB = 4 * x8 + (j & 3), sB = j >> 2;

  const rsrc_t rKB = mk_rsrc(kB + ((size_t)hB * SK + sB * 2048) * 128, 2048 * 256);
  const rsrc_t rVB = mk_rsrc(vB + ((size_t)hB * SK + sB * 2048) * 128, 2048 * 256);
  const rsrc_t rKA = mk_rsrc(kA + ((size_t)x8 * SK + WIN0 + j * 128) * 128, 128 * 256);
  const rsrc_t rVA = mk_rsrc(vA + ((size_t)x8 * SK + WIN0 + j * 128) * 128, 128 * 256);
  const rsrc_t rWQB = mk_rsrc(wqB, 512 * 8192);
  const rsrc_t rWQA = mk_rsrc(wqA, 512 * 8192);
  const rsrc_t rWOB = mk_rsrc(woB, 4096 * 1024);
  const rsrc_t rWOA = mk_rsrc(woA, 4096 * 1024);
  const rsrc_t rXB = mk_rsrc(xB, 1024);
  const rsrc_t rXA = mk_rsrc(xA, 1024);
  const rsrc_t rWS = mk_rsrc(ws, WS_END);

  const unsigned koff = (n >> 2) * 256 + g * 64 + (n & 3) * 16, voff = lane * 16;
  const unsigned wvoff = wv * 4096;

  v4u qa, g1, g2[8], woa[2], wob[16];
  v4u wqb[2], wqa[2];
  unsigned short xbr[2], xar[2];
  const int hAq = 4 * x8 + (j >> 3), ks = j & 7;
#pragma unroll
  for (int t = 0; t < 2; ++t) {
    wqb[t] = ld128<WQ_AUX>(rWQB, g * 8192 + n * 16, (64 * sB + 8 * wv + 4 * t) * 8192 + hB * 256);
    xbr[t] = __builtin_amdgcn_raw_buffer_load_b16(rXB, g * 2, (64 * sB + 8 * wv + 4 * t) * 2, 0);
  }
#pragma unroll
  for (int t = 0; t < 2; ++t) {
    wqa[t] = ld128<WQ_AUX>(rWQA, g * 8192 + n * 16, (64 * ks + 8 * wv + 4 * t) * 8192 + hAq * 256);
    xar[t] = __builtin_amdgcn_raw_buffer_load_b16(rXA, g * 2, (64 * ks + 8 * wv + 4 * t) * 2, 0);
  }
  const unsigned gen = __builtin_amdgcn_raw_buffer_load_b32(rWS, WS_GEN, 0, AUX_SC1);
  unsigned pt[6];
  pt[0] = __builtin_amdgcn_raw_buffer_load_b32(rKA, 0, wvoff, AUX_NT);
  pt[1] = __builtin_amdgcn_raw_buffer_load_b32(rVA, 0, wvoff, AUX_NT);
  pt[2] = __builtin_amdgcn_raw_buffer_load_b32(rWOA, 0, (512 * x8 + 16 * j) * 1024, AUX_NT);
  pt[3] = __builtin_amdgcn_raw_buffer_load_b32(rWOB, 0, (hB * 128 + 16 * wv) * 1024, AUX_NT);
  pt[4] = __builtin_amdgcn_raw_buffer_load_b32(rWS, 0, WS_YA + w * 4096, AUX_SC1);
  pt[5] = __builtin_amdgcn_raw_buffer_load_b32(rWS, 0, WS_YB + w * 4096, AUX_SC1);
  SB();
  v4u K[NSLOT][4], V[NSLOT][4];
  auto issue = [&](int u) __attribute__((always_inline)) {
    const int s = u >> 1;
    const bool isA = s == SA;
    const int tb = s < SA ? s : s - 1;
    if (u & 1) {
#pragma unroll
      for (int k = 0; k < 4; ++k) V[s][k] = ld128<V_AUX>(isA ? rVA : rVB, voff + k * 1024, wvoff + (isA ? 0 : tb * 32768));
    } else {
#pragma unroll
      for (int k = 0; k < 4; ++k) K[s][k] = ld128<K_AUX>(isA ? rKA : rKB, koff + k * 1024, wvoff + (isA ? 0 : tb * 32768));
    }
  };
  if (tid < 4) lds[L_FLAG + tid] = 0.f;
  __builtin_amdgcn_s_barrier();
  issue(0);
  SB();
  const unsigned tag = __builtin_amdgcn_readfirstlane(gen) + 1u;
  v4u mask;
  {
    const unsigned hm = (n & 1) ? 0xffff0000u : 0x0000ffffu;
    const int q = (n & 7) >> 1;
    mask = v4u{q == 0 ? hm : 0u, q == 1 ? hm : 0u, q == 2 ? hm : 0u, q == 3 ? hm : 0u};
  }
  {
    f32x4 DB = {0.f, 0.f, 0.f, 0.f}, DA = {0.f, 0.f, 0.f, 0.f};
#pragma unroll
    for (int t = 0; t < 2; ++t) {
      DB = mfma(wqb[t], andm((unsigned)xbr[t] * 0x10001u, mask), DB);
      DA = mfma(wqa[t], andm((unsigned)xar[t] * 0x10001u, mask), DA);
    }
    if (n < 8) {
      *(f32x4*)&lds[L_QPART + wv * 128 + (g * 8 + n) * 4] = DB;
      *(f32x4*)&lds[L_QPART + 1024 + wv * 128 + (g * 8 + n) * 4] = DA;
    }
  }
  STAMP(24);
  __syncthreads();
  if (wv < 2) {
    const int c = 2 * lane;
    const int e = ((c >> 5) * 8 + (c & 7)) * 4 + ((c >> 3) & 3);
    float y0 = 0.f, y1 = 0.f;
#pragma unroll
    for (int k = 0; k < 8; ++k) { y0 += lds[L_QPART + wv * 1024 + k * 128 + e]; y1 += lds[L_QPART + wv * 1024 + k * 128 + e + 4]; }
    const unsigned dst = wv == 0 ? WS_PQB + (hB * 8 + sB) * 1024 : WS_PQA + (hAq * 8 + ks) * 1024;
    __builtin_amdgcn_raw_buffer_store_b128(v4u{__float_as_uint(y0), tag, __float_as_uint(y1), tag}, rWS, dst + lane * 16, 0, AUX_SC1);
  }
  STAMP(25);
  SB();
  issue(1);
  SB();
  PIN(K[0][3]);
  issue(2);
  SB();
  PIN(V[0][3]);
  STAMP(26);
  {
    const unsigned src = WS_PQB + (hB * 8 + wv) * 1024 + lane * 16;
    v4u pq = ld128<AUX_SC1>(rWS, src, 0);
    SB();
    issue(3);
    SB();
    PIN(K[1][3]);
    STAMP(28);
    issue(4);
    SB();
    AWAIT(__all(tag2(pq, tag)), pq = ld128<AUX_SC1>(rWS, src, 0));
    *(f32x2*)&lds[L_QSUM + wv * 128 + 2 * lane] = f32x2{__uint_as_float(pq.x), __uint_as_float(pq.z)};
  }
  asm volatile("" :: "v"(pt[0]), "v"(pt[1]), "v"(pt[2]), "v"(pt[3]), "v"(pt[4]), "v"(pt[5]));
  STAMP(27);
  __syncthreads();
  v4u qB;
  {
    f32x4 a = {0.f, 0.f, 0.f, 0.f}, b = {0.f, 0.f, 0.f, 0.f};
#pragma unroll
    for (int k = 0; k < 8; ++k) {
      a += *(const f32x4*)&lds[L_QSUM + k * 128 + 8 * (4 * g + (n & 3))];
      b += *(const f32x4*)&lds[L_QSUM + k * 128 + 8 * (4 * g + (n & 3)) + 4];
    }
    qB = v4u{cvtpk(a[0], a[1]), cvtpk(a[2], a[3]), cvtpk(b[0], b[1]), cvtpk(b[2], b[3])};
  }
  v4u Bq[4];
  auto make_bq = [&](const v4u& q) __attribute__((always_inline)) {
#pragma unroll
    for (int u = 0; u < 4; ++u) Bq[u] = ((n >> 2) == u) ? q : v4u{0u, 0u, 0u, 0u};
  };
  make_bq(qB);
  STAMP(1);
  State st;
  st.m = -INFINITY; st.l = 0.f; st.acc = f32x4{0.f, 0.f, 0.f, 0.f};

  auto slot = [&](auto S_) __attribute__((always_inline)) {
    constexpr int s = decltype(S_)::value;
    if constexpr (s != SA || (ABL & 1)) {
      f32x4 S;
      qk_part(K[s], Bq, S);
      PIN(S);
      SB();
      if constexpr (next_unit(2 * s) >= 0) issue(next_unit(2 * s));
      SB();
      sm_pv(S, V[s], mask, n, st);
      PIN(st.acc); PIN(st.l);
      SB();
      if constexpr (next_unit(2 * s + 1) >= 0) issue(next_unit(2 * s + 1));
      SB();
    } else {
      issue(next_unit(2 * s)); issue(next_unit(2 * s + 1));
      SB();
      while (__hip_atomic_load((unsigned*)&lds[L_FLAG + 2], __ATOMIC_RELAXED, __HIP_MEMORY_SCOPE_WORKGROUP) != 2u) __builtin_amdgcn_s_sleep(1);
      MEMFENCE();
      State sa[4];
#pragma unroll
      for (int hd = 0; hd < 4; ++hd) {
        const v4u q = *(const v4u*)&lds[L_QA + hd * 64 + (4 * g + (n & 3)) * 4];
        v4u Ba[4];
#pragma unroll
        for (int u = 0; u < 4; ++u) Ba[u] = ((n >> 2) == u) ? q : v4u{0u, 0u, 0u, 0u};
        sa[hd].m = -INFINITY; sa[hd].l = 0.f; sa[hd].acc = f32x4{0.f, 0.f, 0.f, 0.f};
        f32x4 S;
        qk_part(K[s], Ba, S);
        sm_pv(S, V[s], mask, n, sa[hd]);
        finish_state(sa[hd]);
      }
#pragma unroll
      for (int hd = 0; hd < 4; ++hd) {
        float* rec = &lds[L_PARTA + (wv * 4 + hd) * 132];
        if (n < 8) {
#pragma unroll
          for (int i = 0; i < 4; ++i) rec[4 + 32 * g + 8 * i + n] = sa[hd].acc[i];
        }
        if (lane == 0) { rec[0] = sa[hd].m; rec[1] = sa[hd].l; }
      }
      asm volatile("s_waitcnt lgkmcnt(0)" ::: "memory");
      if (lane == 0) __hip_atomic_fetch_add((unsigned*)&lds[L_FLAG], 1u, __ATOMIC_RELAXED, __HIP_MEMORY_SCOPE_WORKGROUP);
      SB();
    }
    if constexpr (s == S_PA_C && !(ABL & 1)) {
      while (__hip_atomic_load((unsigned*)&lds[L_FLAG], __ATOMIC_RELAXED, __HIP_MEMORY_SCOPE_WORKGROUP) != 8u) __builtin_amdgcn_s_sleep(1);
      MEMFENCE();
      const int hd = wv >> 1, d = 64 * (wv & 1) + lane;
      float M = -INFINITY;
#pragma unroll
      for (int k = 0; k < 8; ++k) M = fmaxf(M, lds[L_PARTA + (k * 4 + hd) * 132]);
      float o = 0.f, L = 0.f;
#pragma unroll
      for (int k = 0; k < 8; ++k) {
        const float* rec = &lds[L_PARTA + (k * 4 + hd) * 132];
        const float f = __builtin_amdgcn_exp2f((rec[0] - M) * C2);
        o = fmaf(f, rec[4 + d], o);
        L = fmaf(f, rec[1], L);
      }
      const unsigned base = WS_PA + ((x8 * 32 + j) * 4 + hd) * PA_REC;
      __builtin_amdgcn_raw_buffer_store_b64(v2u{__float_as_uint(o), tag}, rWS, base + 16 + d * 8, 0, AUX_SC1);
      if (d == 0) __builtin_amdgcn_raw_buffer_store_b128(v4u{__float_as_uint(M), tag, __float_as_uint(L), tag}, rWS, base, 0, AUX_SC1);
      SB();
    }
    STAMP(2 + s);
    if constexpr (s % LOCKSTEP == LOCKSTEP - 1 && s + 1 < NSLOT) { SB(); __builtin_amdgcn_s_barrier(); SB(); }
    if constexpr (s == S_QA_G1 && !(ABL & 1)) if (wv == 4) {
      g1 = ld128<AUX_SC1>(rWS, WS_PQA + (hAq * 8 + (lane >> 3)) * 1024 + (16 * ks + 2 * (lane & 7)) * 8, 0);
      SB();
    }
    if constexpr (s == S_QA_R && !(ABL & 1)) if (wv == 4) {
      AWAIT(__all(tag2(g1, tag)), g1 = ld128<AUX_SC1>(rWS, WS_PQA + (hAq * 8 + (lane >> 3)) * 1024 + (16 * ks + 2 * (lane & 7)) * 8, 0));
      float v0 = __uint_as_float(g1.x), v1 = __uint_as_float(g1.z);
      v0 += dppf<ROR8>(v0); v1 += dppf<ROR8>(v1);
      v0 = xrow_sum(v0); v1 = xrow_sum(v1);
      if (lane < 8) __builtin_amdgcn_raw_buffer_store_b64(v2u{cvtpk(v0, v1), tag}, rWS, WS_QA + (x8 * 32 + j) * 128 + lane * 8, 0, AUX_SC1);
      SB();
    }
    if constexpr (s == S_QA_G2 && !(ABL & 1)) if (wv < 2) {
      qa = ld128<AUX_SC1>(rWS, WS_QA + (x8 * 32 + 16 * wv + (lane >> 2)) * 128 + (lane & 3) * 16, 0);
      SB();
    }
    if constexpr (s == S_QA_L && !(ABL & 1)) if (wv < 2) {
      AWAIT(__all(tag2(qa, tag)), qa = ld128<AUX_SC1>(rWS, WS_QA + (x8 * 32 + 16 * wv + (lane >> 2)) * 128 + (lane & 3) * 16, 0));
      *(v2u*)&lds[L_QA + 2 * (64 * wv + lane)] = v2u{qa.x, qa.z};
      asm volatile("s_waitcnt lgkmcnt(0)" ::: "memory");
      if (lane == 0) __hip_atomic_fetch_add((unsigned*)&lds[L_FLAG + 2], 1u, __ATOMIC_RELAXED, __HIP_MEMORY_SCOPE_WORKGROUP);
      SB();
    }
    if constexpr (s == S_PA_G && !(ABL & 1)) if (wv == 7) {
      const unsigned base = WS_PA + ((x8 * 32 + (lane >> 3)) * 4 + (j >> 3)) * PA_REC;
#pragma unroll
      for (int k = 0; k < 4; ++k) {
        g2[k] = ld128<AUX_SC1>(rWS, base + k * 8 * 4 * PA_REC, 0);
        g2[4 + k] = ld128<AUX_SC1>(rWS, base + k * 8 * 4 * PA_REC + 16 + (16 * (j & 7) + 2 * (lane & 7)) * 8, 0);
      }
      SB();
    }
    if constexpr (s == S_PA_R && !(ABL & 1)) if (wv == 7) {
      const unsigned base = WS_PA + ((x8 * 32 + (lane >> 3)) * 4 + (j >> 3)) * PA_REC;
      auto g2_load = [&]() __attribute__((always_inline)) {
#pragma unroll
        for (int k = 0; k < 4; ++k) {
          g2[k] = ld128<AUX_SC1>(rWS, base + k * 8 * 4 * PA_REC, 0);
          g2[4 + k] = ld128<AUX_SC1>(rWS, base + k * 8 * 4 * PA_REC + 16 + (16 * (j & 7) + 2 * (lane & 7)) * 8, 0);
        }
      };
      AWAIT(__all(tag2(g2[0], tag) && tag2(g2[1], tag) && tag2(g2[2], tag) && tag2(g2[3], tag) && tag2(g2[4], tag) && tag2(g2[5], tag) &&
                  tag2(g2[6], tag) && tag2(g2[7], tag)), g2_load());
      float M = -INFINITY;
#pragma unroll
      for (int k = 0; k < 4; ++k) M = fmaxf(M, __uint_as_float(g2[k].x));
      M = fmaxf(M, dppf<ROR8>(M));
      M = xrow_max(M);
      float L = 0.f, a0 = 0.f, a1 = 0.f;
#pragma unroll
      for (int k = 0; k < 4; ++k) {
        const float f = __builtin_amdgcn_exp2f((__uint_as_float(g2[k].x) - M) * C2);
        L = fmaf(f, __uint_as_float(g2[k].z), L);
        a0 = fmaf(f, __uint_as_float(g2[4 + k].x), a0);
        a1 = fmaf(f, __uint_as_float(g2[4 + k].z), a1);
      }
      L += dppf<ROR8>(L); a0 += dppf<ROR8>(a0); a1 += dppf<ROR8>(a1);
      L = xrow_sum(L); a0 = xrow_sum(a0); a1 = xrow_sum(a1);
      if (lane < 8) *(f32x2*)&lds[L_OFIN + 2 * lane] = f32x2{a0 / L, a1 / L};
      asm volatile("s_waitcnt lgkmcnt(0)" ::: "memory");
      if (lane == 0) __hip_atomic_store((unsigned*)&lds[L_FLAG + 1], 1u, __ATOMIC_RELAXED, __HIP_MEMORY_SCOPE_WORKGROUP);
      SB();
    }
    if constexpr (s == S_YA && !(ABL & 1)) {
      while (__hip_atomic_load((unsigned*)&lds[L_FLAG + 1], __ATOMIC_RELAXED, __HIP_MEMORY_SCOPE_WORKGROUP) != 1u) __builtin_amdgcn_s_sleep(1);
      MEMFENCE();
      f32x4 Y = {0.f, 0.f, 0.f, 0.f};
#pragma unroll
      for (int t = 0; t < 2; ++t) {
        const float o = lds[L_OFIN + 8 * t + 2 * g + (n >> 3)];
        Y = mfma(woa[t], andm(split_rep(o, false), mask), Y);
        Y = mfma(woa[t], andm(split_rep(o, true), mask), Y);
      }
      const bool valid = (g >> 1) == (n >> 3);
      float z[4];
#pragma unroll
      for (int i = 0; i < 4; ++i) {
        float t = valid ? Y[i] : 0.f;
        t += dppf<ROR8>(t);
        auto sw = __builtin_amdgcn_permlane32_swap(__float_as_uint(t), __float_as_uint(t), false, false);
        z[i] = __uint_as_float(sw[0]) + __uint_as_float(sw[1]);
      }
      const int i = 2 * (g >> 1) + (n >> 3);
      const float zi = (i & 2) ? ((i & 1) ? z[3] : z[2]) : ((i & 1) ? z[1] : z[0]);
      __builtin_amdgcn_raw_buffer_store_b64(v2u{__float_as_uint(zi), tag}, rWS,
                                            WS_YA + w * 4096 + (64 * wv + 32 * (g & 1) + 8 * i + (n & 7)) * 8, 0, AUX_SC1);
      SB();
    }
    if constexpr (s >= S_WO && s < S_WO + 8 && !(ABL & 2)) {
#pragma unroll
      for (int k = 2 * (s - S_WO); k < 2 * (s - S_WO) + 2; ++k)
        wob[k] = ld128<WO_AUX>(rWOB, g * 1024 + n * 16, (hB * 128 + 16 * wv + 4 * (k >> 2)) * 1024 + (k & 3) * 256);
      SB();
    }
    if constexpr (s == S_WOA && !(ABL & 1)) {
#pragma unroll
      for (int t = 0; t < 2; ++t)
        woa[t] = ld128<AUX_NT>(rWOA, (lane >> 3) * 1024 + (lane & 7) * 16, (512 * x8 + 16 * j + 8 * t) * 1024 + wv * 128);
      SB();
    }
  };
  run_slots(slot, std::make_integer_sequence<int, NSLOT>{});

  STAMP(19);
  finish_state(st);
  if (n < 8) {
#pragma unroll
    for (int i = 0; i < 4; ++i) lds[L_PARTB + wv * 128 + opos(32 * g + 8 * i + n)] = st.acc[i];
  }
  if (lane == 0) { lds[L_MLB + 2 * wv] = st.m; lds[L_MLB + 2 * wv + 1] = st.l; }
  __syncthreads();
  {
    float M = -INFINITY;
#pragma unroll
    for (int k = 0; k < 8; ++k) M = fmaxf(M, lds[L_MLB + 2 * k]);
    float L = 0.f;
    f32x4 o4 = {0.f, 0.f, 0.f, 0.f};
#pragma unroll
    for (int k = 0; k < 8; ++k) {
      const float f = __builtin_amdgcn_exp2f((lds[L_MLB + 2 * k] - M) * C2);
      L = fmaf(f, lds[L_MLB + 2 * k + 1], L);
      o4 += f * *(const f32x4*)&lds[L_PARTB + k * 128 + 16 * wv + 4 * g];
    }
    if (tid == 0)
      __builtin_amdgcn_raw_buffer_store_b128(v4u{__float_as_uint(M), tag, __float_as_uint(L), tag}, rWS, WS_MLB + (hB * 8 + sB) * 128, 0, AUX_SC1);
    f32x4 Y[4];
#pragma unroll
    for (int cb = 0; cb < 4; ++cb) Y[cb] = f32x4{0.f, 0.f, 0.f, 0.f};
#pragma unroll
    for (int rg = 0; rg < 4; ++rg) {
      const v4u fr = andm(split_rep(o4[rg], n >= 8), mask);
#pragma unroll
      for (int cb = 0; cb < 4; ++cb) Y[cb] = mfma(wob[rg * 4 + cb], fr, Y[cb]);
    }
#pragma unroll
    for (int cb = 0; cb < 4; ++cb) {
#pragma unroll
      for (int i = 0; i < 4; ++i) Y[cb][i] += dppf<ROR8>(Y[cb][i]);
      if (n < 8) *(f32x4*)&lds[L_YPART + wv * 512 + ((cb * 4 + g) * 8 + n) * 4] = Y[cb];
    }
  }
  __syncthreads();
  STAMP(20);
  if (tid < 256) {
    const int c = 2 * tid;
    const int idx = (((c >> 7) * 4 + ((c >> 5) & 3)) * 8 + (c & 7)) * 4 + ((c >> 3) & 3);
    float y0 = 0.f, y1 = 0.f;
#pragma unroll
    for (int k = 0; k < 8; ++k) { y0 += lds[L_YPART + k * 512 + idx]; y1 += lds[L_YPART + k * 512 + idx + 4]; }
    __builtin_amdgcn_raw_buffer_store_b128(v4u{__float_as_uint(y0), tag, __float_as_uint(y1), tag}, rWS,
                                           WS_YB + (hB * 8 + sB) * 4096 + tid * 16, 0, AUX_SC1);
  }
  STAMP(21);
  {
    const bool isB = wv < 4;
    const unsigned yoff = (isB ? WS_YB : WS_YA) + (tid & 255) * 4096 + w * 16;
    v4u y, ml = v4u{0u, tag, 0u, tag};
    for (unsigned spins = 0;; ++spins) {
      MEMFENCE();
      y = ld128<AUX_SC1>(rWS, yoff, 0);
      if (isB) ml = ld128<AUX_SC1>(rWS, WS_MLB + tid * 128, 0);
#define DBGV (tag - y.y)
      GIVEUP(3, tag2(y, tag));
      GIVEUP(4, tag2(ml, tag));
#undef DBGV
      if (__all(tag2(y, tag) && tag2(ml, tag)) || spins > SPIN_CAP || ((ABL & 1) && !isB)) break;
      __builtin_amdgcn_s_sleep(1);
    }
    STAMP(22);
    float v0 = __uint_as_float(y.x), v1 = __uint_as_float(y.z);
    if (isB) {
      const float m = __uint_as_float(ml.x);
      float M = fmaxf(m, dppf<XOR1>(m));
      M = fmaxf(M, dppf<XOR2>(M));
      M = fmaxf(M, dppf<HMIRROR>(M));
      const float f = __builtin_amdgcn_exp2f((m - M) * C2);
      float L = f * __uint_as_float(ml.z);
      L += dppf<XOR1>(L); L += dppf<XOR2>(L); L += dppf<HMIRROR>(L);
      const float c = f / L;
      v0 *= c; v1 *= c;
    }
    v0 += dppf<XOR1>(v0); v0 += dppf<XOR2>(v0); v0 += dppf<ROR4>(v0); v0 += dppf<ROR8>(v0); v0 = xrow_sum(v0);
    v1 += dppf<XOR1>(v1); v1 += dppf<XOR2>(v1); v1 += dppf<ROR4>(v1); v1 += dppf<ROR8>(v1); v1 = xrow_sum(v1);
    if (lane == 0) { lds[L_RED + 2 * wv] = v0; lds[L_RED + 2 * wv + 1] = v1; }
  }
  __syncthreads();
  if (lane == 0 && (wv == 0 || wv == 4)) {
    const float* r = &lds[L_RED + 2 * wv];
    const float s0 = (r[0] + r[2]) + (r[4] + r[6]), s1 = (r[1] + r[3]) + (r[5] + r[7]);
    *(unsigned*)(out + (wv == 0 ? 512 : 0) + 2 * w) = cvtpk(s0, s1);
    if (w == 0 && wv == 0) __builtin_amdgcn_raw_buffer_store_b32(tag, rWS, WS_GEN, 0, AUX_SC1);
  }
}

}

extern "C" void kernel_launch(void* const* d_in, const int* in_sizes, int n_in,
                              void* d_out, int out_size, void* d_ws, size_t ws_size,
                              hipStream_t stream) {
  (void)in_sizes; (void)n_in; (void)out_size; (void)ws_size;
  fused_decode<<<256, 512, 0, stream>>>(
      (const uint16_t*)d_in[0], (const uint16_t*)d_in[1], (const uint16_t*)d_in[2], (const uint16_t*)d_in[3], (const uint16_t*)d_in[4],
      (const uint16_t*)d_in[5], (const uint16_t*)d_in[6], (const uint16_t*)d_in[7], (const uint16_t*)d_in[8], (const uint16_t*)d_in[9],
      (uint16_t*)d_out, (unsigned char*)d_ws);
}
```

```cpp
#include <hip/hip_runtime.h>
#include <hip/hip_bf16.h>
#include <stdint.h>
#include <utility>

#define GAS __attribute__((address_space(1)))
typedef unsigned v4u __attribute__((ext_vector_type(4)));
typedef unsigned v2u __attribute__((ext_vector_type(2)));
typedef float f32x4 __attribute__((ext_vector_type(4)));
typedef float f32x2 __attribute__((ext_vector_type(2)));
typedef short bf16x8 __attribute__((ext_vector_type(8)));
typedef __bf16 bf16x2_t __attribute__((ext_vector_type(2)));
typedef __amdgpu_buffer_rsrc_t rsrc_t;

namespace {

constexpr int AUX_NT = 2, AUX_SC1 = 16;
constexpr int RSRC_W3 = 0x00020000;
constexpr float SCALE = 0.08838834764831845f;
constexpr float C2 = 1.4426950408889634f;
constexpr int SK = 16384, WIN0 = 16384 - 4096;
constexpr int NSLOT = 17;
#define DIST 4
constexpr int PRE = 5;
constexpr int next_unit(int u) { return (u + DIST >= PRE && u + DIST < 2 * NSLOT) ? u + DIST : -1; }
#define NBUF 2
constexpr int S_QA_G1 = 0, S_QA_R = S_QA_G1 + NBUF;
constexpr int S_QA_G2 = 5, S_QA_L = S_QA_G2 + NBUF;
constexpr int SA = S_QA_L + 1;
constexpr int S_PA_C = SA + 2;
constexpr int S_PA_G = 12, S_PA_R = S_PA_G + NBUF;
constexpr int S_WO = 5, S_WOA = 13;
constexpr int S_YA = S_PA_R + 1;
constexpr unsigned SPIN_CAP = 1u << 18;

constexpr unsigned WS_GEN = 0;
constexpr unsigned WS_PQB = 4096;
constexpr unsigned WS_PQA = WS_PQB + 256 * 1024;
constexpr unsigned WS_QA = WS_PQA + 256 * 1024;
constexpr unsigned PA_REC = 1152;
constexpr unsigned WS_PA = WS_QA + 8 * 32 * 128;
constexpr unsigned WS_MLB = WS_PA + 8 * 32 * 4 * PA_REC;
constexpr unsigned WS_PB = WS_MLB + 256 * 128;
constexpr unsigned WS_YA = WS_PB + 256 * PA_REC;
constexpr unsigned WS_YB = WS_YA + (1u << 20);
constexpr unsigned WS_END = WS_YB + (1u << 20);
#define ABL 0
#define PRETOUCH 1
#define PACE_TAU 0
#define PACE_T0 800
#define END_SPIN 0
#define TAIL2 0
#define POLL_SLEEP 4
#define LOCKSTEP 2
#define PRO_MODE 2
#define K_AUX 2
#define V_AUX 2
#define WQ_AUX 2
#define WA_AUX 0
#define WO_AUX 2

constexpr int L_QPART = 0;
constexpr int L_QSUM = L_QPART + 2 * 8 * 128;
constexpr int L_PARTA = L_QSUM + 8 * 128;
constexpr int L_PARTB = L_PARTA + 8 * 4 * 132;
constexpr int L_MLB = L_PARTB + 8 * 128;
constexpr int L_YPART = L_MLB + 16;
constexpr int L_RED = L_YPART + 8 * 512;
constexpr int L_OFIN = L_RED + 16;
constexpr int L_OFB = L_OFIN + 16;
constexpr int L_QA = L_OFB + 128;
constexpr int L_FLAG = L_QA + 256;
constexpr int L_END = L_FLAG + 4;

__device__ __forceinline__ rsrc_t mk_rsrc(const void* p, unsigned bytes) {
  return __builtin_amdgcn_make_buffer_rsrc(const_cast<void*>(p), 0, bytes, RSRC_W3);
}
template <int AUX> __device__ __forceinline__ v4u ld128(rsrc_t rs, unsigned voff, unsigned soff) {
  return __builtin_amdgcn_raw_buffer_load_b128(rs, voff, soff, AUX);
}
__device__ __forceinline__ f32x4 mfma(v4u a, v4u b, f32x4 c) {
  return __builtin_amdgcn_mfma_f32_16x16x32_bf16(__builtin_bit_cast(bf16x8, a), __builtin_bit_cast(bf16x8, b), c, 0, 0, 0);
}
__device__ __forceinline__ unsigned cvtpk(float lo, float hi) {
  f32x2 v = {lo, hi};
  return __builtin_bit_cast(unsigned, __builtin_convertvector(v, bf16x2_t));
}
template <int CTRL> __device__ __forceinline__ float dppf(float x) {
  return __builtin_bit_cast(float, __builtin_amdgcn_mov_dpp(__builtin_bit_cast(int, x), CTRL, 0xf, 0xf, true));
}
template <int CTRL, int RM, int BM> __device__ __forceinline__ unsigned dppu(unsigned old, unsigned x) {
  return (unsigned)__builtin_amdgcn_update_dpp((int)old, (int)x, CTRL, RM, BM, false);
}
constexpr int XOR1 = 0xB1, XOR2 = 0x4E, HMIRROR = 0x141, MIRROR = 0x140, ROR8 = 0x128, ROR4 = 0x124, BCAST = 0x150;
__device__ __forceinline__ float xrow_max(float x) {
  auto s = __builtin_amdgcn_permlane16_swap(__float_as_uint(x), __float_as_uint(x), false, false);
  x = fmaxf(__uint_as_float(s[0]), __uint_as_float(s[1]));
  auto t = __builtin_amdgcn_permlane32_swap(__float_as_uint(x), __float_as_uint(x), false, false);
  return fmaxf(__uint_as_float(t[0]), __uint_as_float(t[1]));
}
__device__ __forceinline__ float xrow_sum(float x) {
  auto s = __builtin_amdgcn_permlane16_swap(__float_as_uint(x), __float_as_uint(x), false, false);
  x = __uint_as_float(s[0]) + __uint_as_float(s[1]);
  auto t = __builtin_amdgcn_permlane32_swap(__float_as_uint(x), __float_as_uint(x), false, false);
  return __uint_as_float(t[0]) + __uint_as_float(t[1]);
}
__device__ __forceinline__ v4u andm(unsigned rep, const v4u& m) { return v4u{rep & m.x, rep & m.y, rep & m.z, rep & m.w}; }
__device__ __forceinline__ unsigned split_rep(float x, bool take_lo) {
  const unsigned hi = cvtpk(x, x);
  const float r = x - __uint_as_float(hi & 0xffff0000u);
  const unsigned lo = cvtpk(r, r);
  return take_lo ? lo : hi;
}
#define STAMP(k)
#define GIVEUP(code, okv)
#define SB() __builtin_amdgcn_sched_barrier(0)
#define MEMFENCE() asm volatile("" ::: "memory")
#define AWAIT(OK, RELOAD) do { if (__builtin_expect(!(OK), 0)) { unsigned spins_ = 0; \
    do { __builtin_amdgcn_s_sleep(2); MEMFENCE(); RELOAD; } while (!(OK) && ++spins_ < SPIN_CAP); } } while (0)
#define PIN(x) asm volatile("" : "+v"(x) :: "memory")

template <typename F, int... I>
__device__ __forceinline__ void run_slots(F& f, std::integer_sequence<int, I...>) { (f(std::integral_constant<int, I>{}), ...); }

struct State { float m, l; f32x4 acc; };

__device__ __forceinline__ void qk_part(const v4u (&K)[4], const v4u (&Bq)[4], f32x4& S) {
  S = f32x4{0.f, 0.f, 0.f, 0.f};
#pragma unroll
  for (int u = 0; u < 4; ++u) S = mfma(K[u], Bq[u], S);
}
__device__ __forceinline__ void sm_pv(const f32x4& S, const v4u (&V)[4], const v4u& mask, int n, State& st) {
  const float s01 = (n & 1) ? S[1] : S[0], s23 = (n & 1) ? S[3] : S[2];
  float s = (n & 2) ? s23 : s01;
  s += dppf<XOR1>(s);
  s += dppf<XOR2>(s);
  s = __uint_as_float(cvtpk(s, s) & 0xffff0000u) * SCALE;
  s = __uint_as_float(cvtpk(s, s) & 0xffff0000u);
  float tm = fmaxf(s, dppf<ROR4>(s));
  tm = fmaxf(tm, dppf<ROR8>(tm));
  tm = xrow_max(tm);
  const float mn = fmaxf(st.m, tm);
  const float alpha = __builtin_amdgcn_exp2f((st.m - mn) * C2);
  const float p = __builtin_amdgcn_exp2f((s - mn) * C2);
  st.m = mn;
  st.l = fmaf(st.l, alpha, p);
  st.acc *= alpha;
  const unsigned pk = split_rep(p, (n & 2) != 0);
  unsigned rep;
#define PV_STEP(u) rep = dppu<BCAST + 4 * (u), 0xf, 0x3>(pk, pk); rep = dppu<BCAST + 4 * (u) + 2, 0xf, 0xc>(rep, pk); \
  st.acc = mfma(V[u], andm(rep, mask), st.acc);
  PV_STEP(0) PV_STEP(1) PV_STEP(2) PV_STEP(3)
#undef PV_STEP
}
__device__ __forceinline__ void finish_state(State& st) {
  float l = st.l;
  l += dppf<ROR4>(l);
  l += dppf<ROR8>(l);
  st.l = xrow_sum(l);
#pragma unroll
  for (int i = 0; i < 4; ++i) st.acc[i] += dppf<ROR8>(st.acc[i]);
}
__device__ __forceinline__ int opos(int d) { return (d >> 4) * 16 + (d & 3) * 4 + ((d >> 2) & 3); }
__device__ __forceinline__ bool tag2(const v4u& x, unsigned tag) { return x.y == tag && x.w == tag; }

__global__ void __launch_bounds__(512, 2)
fused_decode(const uint16_t* __restrict__ xA, const uint16_t* __restrict__ kA, const uint16_t* __restrict__ vA,
             const uint16_t* __restrict__ wqA, const uint16_t* __restrict__ woA,
             const uint16_t* __restrict__ xB, const uint16_t* __restrict__ kB, const uint16_t* __restrict__ vB,
             const uint16_t* __restrict__ wqB, const uint16_t* __restrict__ woB,
             uint16_t* __restrict__ out, unsigned char* ws) {
  __shared__ __attribute__((aligned(16))) float lds[L_END];
  const int tid = threadIdx.x, lane = tid & 63, wv = __builtin_amdgcn_readfirstlane(tid >> 6);
  const int g = lane >> 4, n = lane & 15;
  STAMP(0);
  const int w = blockIdx.x, x8 = w & 7, j = w >> 3;
  const int hB = 4 * x8 + (j & 3), sB = j >> 2;

  const rsrc_t rKB = mk_rsrc(kB + ((size_t)hB * SK + sB * 2048) * 128, 2048 * 256);
  const rsrc_t rVB = mk_rsrc(vB + ((size_t)hB * SK + sB * 2048) * 128, 2048 * 256);
  const rsrc_t rKA = mk_rsrc(kA + ((size_t)x8 * SK + WIN0 + j * 128) * 128, 128 * 256);
  const rsrc_t rVA = mk_rsrc(vA + ((size_t)x8 * SK + WIN0 + j * 128) * 128, 128 * 256);
  const rsrc_t rWQB = mk_rsrc(wqB, 512 * 8192);
  const rsrc_t rWQA = mk_rsrc(wqA, 512 * 8192);
  const rsrc_t rWOB = mk_rsrc(woB, 4096 * 1024);
  const rsrc_t rWOA = mk_rsrc(woA, 4096 * 1024);
  const rsrc_t rXB = mk_rsrc(xB, 1024);
  const rsrc_t rXA = mk_rsrc(xA, 1024);
  const rsrc_t rWS = mk_rsrc(ws, WS_END);

  const unsigned koff = (n >> 2) * 256 + g * 64 + (n & 3) * 16, voff = lane * 16;
  const unsigned wvoff = wv * 4096;

  v4u qa, g1, g2[8], woa[2], wob[16];
  v4u wqb[2], wqa[2];
  unsigned short xbr[2], xar[2];
  const int hAq = 4 * x8 + (j >> 3), ks = j & 7;
#pragma unroll
  for (int t = 0; t < 2; ++t) {
    wqb[t] = ld128<WQ_AUX>(rWQB, g * 8192 + n * 16, (64 * sB + 8 * wv + 4 * t) * 8192 + hB * 256);
    xbr[t] = __builtin_amdgcn_raw_buffer_load_b16(rXB, g * 2, (64 * sB + 8 * wv + 4 * t) * 2, 0);
  }
#pragma unroll
  for (int t = 0; t < 2; ++t) {
    wqa[t] = ld128<WQ_AUX>(rWQA, g * 8192 + n * 16, (64 * ks + 8 * wv + 4 * t) * 8192 + hAq * 256);
    xar[t] = __builtin_amdgcn_raw_buffer_load_b16(rXA, g * 2, (64 * ks + 8 * wv + 4 * t) * 2, 0);
  }
  const unsigned gen = __builtin_amdgcn_raw_buffer_load_b32(rWS, WS_GEN, 0, AUX_SC1);
  unsigned pt[6];
  pt[0] = __builtin_amdgcn_raw_buffer_load_b32(rKA, 0, wvoff, AUX_NT);
  pt[1] = __builtin_amdgcn_raw_buffer_load_b32(rVA, 0, wvoff, AUX_NT);
  pt[2] = __builtin_amdgcn_raw_buffer_load_b32(rWOA, 0, (512 * x8 + 16 * j) * 1024, AUX_NT);
  pt[3] = __builtin_amdgcn_raw_buffer_load_b32(rWOB, 0, (hB * 128 + 16 * wv) * 1024, AUX_NT);
  pt[4] = __builtin_amdgcn_raw_buffer_load_b32(rWS, 0, WS_YA + w * 4096, AUX_SC1);
  pt[5] = __builtin_amdgcn_raw_buffer_load_b32(rWS, 0, WS_YB + w * 4096, AUX_SC1);
  SB();
  v4u K[NSLOT][4], V[NSLOT][4];
  auto issue = [&](int u) __attribute__((always_inline)) {
    const int s = u >> 1;
    const bool isA = s == SA;
    const int tb = s < SA ? s : s - 1;
    if (u & 1) {
#pragma unroll
      for (int k = 0; k < 4; ++k) V[s][k] = ld128<V_AUX>(isA ? rVA : rVB, voff + k * 1024, wvoff + (isA ? 0 : tb * 32768));
    } else {
#pragma unroll
      for (int k = 0; k < 4; ++k) K[s][k] = ld128<K_AUX>(isA ? rKA : rKB, koff + k * 1024, wvoff + (isA ? 0 : tb * 32768));
    }
  };
  if (tid < 4) lds[L_FLAG + tid] = 0.f;
  __builtin_amdgcn_s_barrier();
  issue(0);
  SB();
  const unsigned tag = __builtin_amdgcn_readfirstlane(gen) + 1u;
  v4u mask;
  {
    const unsigned hm = (n & 1) ? 0xffff0000u : 0x0000ffffu;
    const int q = (n & 7) >> 1;
    mask = v4u{q == 0 ? hm : 0u, q == 1 ? hm : 0u, q == 2 ? hm : 0u, q == 3 ? hm : 0u};
  }
  {
    f32x4 DB = {0.f, 0.f, 0.f, 0.f}, DA = {0.f, 0.f, 0.f, 0.f};
#pragma unroll
    for (int t = 0; t < 2; ++t) {
      DB = mfma(wqb[t], andm((unsigned)xbr[t] * 0x10001u, mask), DB);
      DA = mfma(wqa[t], andm((unsigned)xar[t] * 0x10001u, mask), DA);
    }
    if (n < 8) {
      *(f32x4*)&lds[L_QPART + wv * 128 + (g * 8 + n) * 4] = DB;
      *(f32x4*)&lds[L_QPART + 1024 + wv * 128 + (g * 8 + n) * 4] = DA;
    }
  }
  STAMP(24);
  __syncthreads();
  if (wv < 2) {
    const int c = 2 * lane;
    const int e = ((c >> 5) * 8 + (c & 7)) * 4 + ((c >> 3) & 3);
    float y0 = 0.f, y1 = 0.f;
#pragma unroll
    for (int k = 0; k < 8; ++k) { y0 += lds[L_QPART + wv * 1024 + k * 128 + e]; y1 += lds[L_QPART + wv * 1024 + k * 128 + e + 4]; }
    const unsigned dst = wv == 0 ? WS_PQB + (hB * 8 + sB) * 1024 : WS_PQA + (hAq * 8 + ks) * 1024;
    __builtin_amdgcn_raw_buffer_store_b128(v4u{__float_as_uint(y0), tag, __float_as_uint(y1), tag}, rWS, dst + lane * 16, 0, AUX_SC1);
  }
  STAMP(25);
  SB();
  issue(1);
  SB();
  PIN(K[0][3]);
  issue(2);
  SB();
  PIN(V[0][3]);
  STAMP(26);
  {
    const unsigned src = WS_PQB + (hB * 8 + wv) * 1024 + lane * 16;
    v4u pq = ld128<AUX_SC1>(rWS, src, 0);
    SB();
    issue(3);
    SB();
    PIN(K[1][3]);
    STAMP(28);
    issue(4);
    SB();
    AWAIT(__all(tag2(pq, tag)), pq = ld128<AUX_SC1>(rWS, src, 0));
    *(f32x2*)&lds[L_QSUM + wv * 128 + 2 * lane] = f32x2{__uint_as_float(pq.x), __uint_as_float(pq.z)};
  }
  asm volatile("" :: "v"(pt[0]), "v"(pt[1]), "v"(pt[2]), "v"(pt[3]), "v"(pt[4]), "v"(pt[5]));
  STAMP(27);
  __syncthreads();
  v4u qB;
  {
    f32x4 a = {0.f, 0.f, 0.f, 0.f}, b = {0.f, 0.f, 0.f, 0.f};
#pragma unroll
    for (int k = 0; k < 8; ++k) {
      a += *(const f32x4*)&lds[L_QSUM + k * 128 + 8 * (4 * g + (n & 3))];
      b += *(const f32x4*)&lds[L_QSUM + k * 128 + 8 * (4 * g + (n & 3)) + 4];
    }
    qB = v4u{cvtpk(a[0], a[1]), cvtpk(a[2], a[3]), cvtpk(b[0], b[1]), cvtpk(b[2], b[3])};
  }
  v4u Bq[4];
  auto make_bq = [&](const v4u& q) __attribute__((always_inline)) {
#pragma unroll
    for (int u = 0; u < 4; ++u) Bq[u] = ((n >> 2) == u) ? q : v4u{0u, 0u, 0u, 0u};
  };
  make_bq(qB);
  STAMP(1);
  State st;
  st.m = -INFINITY; st.l = 0.f; st.acc = f32x4{0.f, 0.f, 0.f, 0.f};

  auto outproj64 = [&](const float* of, const v4u (&wr)[2], unsigned ydst) __attribute__((always_inline)) {
    f32x4 Y = {0.f, 0.f, 0.f, 0.f};
#pragma unroll
    for (int t = 0; t < 2; ++t) {
      const float o = of[8 * t + 2 * g + (n >> 3)];
      Y = mfma(wr[t], andm(split_rep(o, false), mask), Y);
      Y = mfma(wr[t], andm(split_rep(o, true), mask), Y);
    }
    const bool valid = (g >> 1) == (n >> 3);
    float z[4];
#pragma unroll
    for (int i = 0; i < 4; ++i) {
      float t = valid ? Y[i] : 0.f;
      t += dppf<ROR8>(t);
      auto sw = __builtin_amdgcn_permlane32_swap(__float_as_uint(t), __float_as_uint(t), false, false);
      z[i] = __uint_as_float(sw[0]) + __uint_as_float(sw[1]);
    }
    const int i = 2 * (g >> 1) + (n >> 3);
    const float zi = (i & 2) ? ((i & 1) ? z[3] : z[2]) : ((i & 1) ? z[1] : z[0]);
    __builtin_amdgcn_raw_buffer_store_b64(v2u{__float_as_uint(zi), tag}, rWS, ydst + (64 * wv + 32 * (g & 1) + 8 * i + (n & 7)) * 8, 0, AUX_SC1);
  };

  auto slot = [&](auto S_) __attribute__((always_inline)) {
    constexpr int s = decltype(S_)::value;
    if constexpr (s != SA || (ABL & 1)) {
      f32x4 S;
      qk_part(K[s], Bq, S);
      PIN(S);
      SB();
      if constexpr (next_unit(2 * s) >= 0) issue(next_unit(2 * s));
      SB();
      sm_pv(S, V[s], mask, n, st);
      PIN(st.acc); PIN(st.l);
      SB();
      if constexpr (next_unit(2 * s + 1) >= 0) issue(next_unit(2 * s + 1));
      SB();
    } else {
      issue(next_unit(2 * s)); issue(next_unit(2 * s + 1));
      SB();
      while (__hip_atomic_load((unsigned*)&lds[L_FLAG + 2], __ATOMIC_RELAXED, __HIP_MEMORY_SCOPE_WORKGROUP) != 2u) __builtin_amdgcn_s_sleep(1);
      MEMFENCE();
      State sa[4];
#pragma unroll
      for (int hd = 0; hd < 4; ++hd) {
        const v4u q = *(const v4u*)&lds[L_QA + hd * 64 + (4 * g + (n & 3)) * 4];
        v4u Ba[4];
#pragma unroll
        for (int u = 0; u < 4; ++u) Ba[u] = ((n >> 2) == u) ? q : v4u{0u, 0u, 0u, 0u};
        sa[hd].m = -INFINITY; sa[hd].l = 0.f; sa[hd].acc = f32x4{0.f, 0.f, 0.f, 0.f};
        f32x4 S;
        qk_part(K[s], Ba, S);
        sm_pv(S, V[s], mask, n, sa[hd]);
        finish_state(sa[hd]);
      }
#pragma unroll
      for (int hd = 0; hd < 4; ++hd) {
        float* rec = &lds[L_PARTA + (wv * 4 + hd) * 132];
        if (n < 8) {
#pragma unroll
          for (int i = 0; i < 4; ++i) rec[4 + 32 * g + 8 * i + n] = sa[hd].acc[i];
        }
        if (lane == 0) { rec[0] = sa[hd].m; rec[1] = sa[hd].l; }
      }
      asm volatile("s_waitcnt lgkmcnt(0)" ::: "memory");
      if (lane == 0) __hip_atomic_fetch_add((unsigned*)&lds[L_FLAG], 1u, __ATOMIC_RELAXED, __HIP_MEMORY_SCOPE_WORKGROUP);
      SB();
    }
    if constexpr (s == S_PA_C && !(ABL & 1)) {
      while (__hip_atomic_load((unsigned*)&lds[L_FLAG], __ATOMIC_RELAXED, __HIP_MEMORY_SCOPE_WORKGROUP) != 8u) __builtin_amdgcn_s_sleep(1);
      MEMFENCE();
      const int hd = wv >> 1, d = 64 * (wv & 1) + lane;
      float M = -INFINITY;
#pragma unroll
      for (int k = 0; k < 8; ++k) M = fmaxf(M, lds[L_PARTA + (k * 4 + hd) * 132]);
      float o = 0.f, L = 0.f;
#pragma unroll
      for (int k = 0; k < 8; ++k) {
        const float* rec = &lds[L_PARTA + (k * 4 + hd) * 132];
        const float f = __builtin_amdgcn_exp2f((rec[0] - M) * C2);
        o = fmaf(f, rec[4 + d], o);
        L = fmaf(f, rec[1], L);
      }
      const unsigned base = WS_PA + ((x8 * 32 + j) * 4 + hd) * PA_REC;
      __builtin_amdgcn_raw_buffer_store_b64(v2u{__float_as_uint(o), tag}, rWS, base + 16 + d * 8, 0, AUX_SC1);
      if (d == 0) __builtin_amdgcn_raw_buffer_store_b128(v4u{__float_as_uint(M), tag, __float_as_uint(L), tag}, rWS, base, 0, AUX_SC1);
      SB();
    }
    STAMP(2 + s);
    if constexpr (s % LOCKSTEP == LOCKSTEP - 1 && s + 1 < NSLOT) { SB(); __builtin_amdgcn_s_barrier(); SB(); }
    if constexpr (s == S_QA_G1 && !(ABL & 1)) if (wv == 4) {
      g1 = ld128<AUX_SC1>(rWS, WS_PQA + (hAq * 8 + (lane >> 3)) * 1024 + (16 * ks + 2 * (lane & 7)) * 8, 0);
      SB();
    }
    if constexpr (s == S_QA_R && !(ABL & 1)) if (wv == 4) {
      AWAIT(__all(tag2(g1, tag)), g1 = ld128<AUX_SC1>(rWS, WS_PQA + (hAq * 8 + (lane >> 3)) * 1024 + (16 * ks + 2 * (lane & 7)) * 8, 0));
      float v0 = __uint_as_float(g1.x), v1 = __uint_as_float(g1.z);
      v0 += dppf<ROR8>(v0); v1 += dppf<ROR8>(v1);
      v0 = xrow_sum(v0); v1 = xrow_sum(v1);
      if (lane < 8) __builtin_amdgcn_raw_buffer_store_b64(v2u{cvtpk(v0, v1), tag}, rWS, WS_QA + (x8 * 32 + j) * 128 + lane * 8, 0, AUX_SC1);
      SB();
    }
    if constexpr (s == S_QA_G2 && !(ABL & 1)) if (wv < 2) {
      qa = ld128<AUX_SC1>(rWS, WS_QA + (x8 * 32 + 16 * wv + (lane >> 2)) * 128 + (lane & 3) * 16, 0);
      SB();
    }
    if constexpr (s == S_QA_L && !(ABL & 1)) if (wv < 2) {
      AWAIT(__all(tag2(qa, tag)), qa = ld128<AUX_SC1>(rWS, WS_QA + (x8 * 32 + 16 * wv + (lane >> 2)) * 128 + (lane & 3) * 16, 0));
      *(v2u*)&lds[L_QA + 2 * (64 * wv + lane)] = v2u{qa.x, qa.z};
      asm volatile("s_waitcnt lgkmcnt(0)" ::: "memory");
      if (lane == 0) __hip_atomic_fetch_add((unsigned*)&lds[L_FLAG + 2], 1u, __ATOMIC_RELAXED, __HIP_MEMORY_SCOPE_WORKGROUP);
      SB();
    }
    if constexpr (s == S_PA_G && !(ABL & 1)) if (wv == 7) {
      const unsigned base = WS_PA + ((x8 * 32 + (lane >> 3)) * 4 + (j >> 3)) * PA_REC;
#pragma unroll
      for (int k = 0; k < 4; ++k) {
        g2[k] = ld128<AUX_SC1>(rWS, base + k * 8 * 4 * PA_REC, 0);
        g2[4 + k] = ld128<AUX_SC1>(rWS, base + k * 8 * 4 * PA_REC + 16 + (16 * (j & 7) + 2 * (lane & 7)) * 8, 0);
      }
      SB();
    }
    if constexpr (s == S_PA_R && !(ABL & 1)) if (wv == 7) {
      const unsigned base = WS_PA + ((x8 * 32 + (lane >> 3)) * 4 + (j >> 3)) * PA_REC;
      auto g2_load = [&]() __attribute__((always_inline)) {
#pragma unroll
        for (int k = 0; k < 4; ++k) {
          g2[k] = ld128<AUX_SC1>(rWS, base + k * 8 * 4 * PA_REC, 0);
          g2[4 + k] = ld128<AUX_SC1>(rWS, base + k * 8 * 4 * PA_REC + 16 + (16 * (j & 7) + 2 * (lane & 7)) * 8, 0);
        }
      };
      AWAIT(__all(tag2(g2[0], tag) && tag2(g2[1], tag) && tag2(g2[2], tag) && tag2(g2[3], tag) && tag2(g2[4], tag) && tag2(g2[5], tag) &&
                  tag2(g2[6], tag) && tag2(g2[7], tag)), g2_load());
      float M = -INFINITY;
#pragma unroll
      for (int k = 0; k < 4; ++k) M = fmaxf(M, __uint_as_float(g2[k].x));
      M = fmaxf(M, dppf<ROR8>(M));
      M = xrow_max(M);
      float L = 0.f, a0 = 0.f, a1 = 0.f;
#pragma unroll
      for (int k = 0; k < 4; ++k) {
        const float f = __builtin_amdgcn_exp2f((__uint_as_float(g2[k].x) - M) * C2);
        L = fmaf(f, __uint_as_float(g2[k].z), L);
        a0 = fmaf(f, __uint_as_float(g2[4 + k].x), a0);
        a1 = fmaf(f, __uint_as_float(g2[4 + k].z), a1);
      }
      L += dppf<ROR8>(L); a0 += dppf<ROR8>(a0); a1 += dppf<ROR8>(a1);
      L = xrow_sum(L); a0 = xrow_sum(a0); a1 = xrow_sum(a1);
      if (lane < 8) *(f32x2*)&lds[L_OFIN + 2 * lane] = f32x2{a0 / L, a1 / L};
      asm volatile("s_waitcnt lgkmcnt(0)" ::: "memory");
      if (lane == 0) __hip_atomic_store((unsigned*)&lds[L_FLAG + 1], 1u, __ATOMIC_RELAXED, __HIP_MEMORY_SCOPE_WORKGROUP);
      SB();
    }
    if constexpr (s == S_YA && !(ABL & 1)) {
      while (__hip_atomic_load((unsigned*)&lds[L_FLAG + 1], __ATOMIC_RELAXED, __HIP_MEMORY_SCOPE_WORKGROUP) != 1u) __builtin_amdgcn_s_sleep(1);
      MEMFENCE();
      outproj64(&lds[L_OFIN], woa, WS_YA + w * 4096);
      SB();
    }
    if constexpr (TAIL2 && s == S_WOA) {
#pragma unroll
      for (int t = 0; t < 2; ++t)
        wob[t] = ld128<AUX_NT>(rWOB, (lane >> 3) * 1024 + (lane & 7) * 16, (hB * 128 + 16 * sB + 8 * t) * 1024 + wv * 128);
      SB();
    }
    if constexpr (!TAIL2 && s >= S_WO && s < S_WO + 8 && !(ABL & 2)) {
#pragma unroll
      for (int k = 2 * (s - S_WO); k < 2 * (s - S_WO) + 2; ++k)
        wob[k] = ld128<WO_AUX>(rWOB, g * 1024 + n * 16, (hB * 128 + 16 * wv + 4 * (k >> 2)) * 1024 + (k & 3) * 256);
      SB();
    }
    if constexpr (s == S_WOA && !(ABL & 1)) {
#pragma unroll
      for (int t = 0; t < 2; ++t)
        woa[t] = ld128<AUX_NT>(rWOA, (lane >> 3) * 1024 + (lane & 7) * 16, (512 * x8 + 16 * j + 8 * t) * 1024 + wv * 128);
      SB();
    }
  };
  run_slots(slot, std::make_integer_sequence<int, NSLOT>{});

  STAMP(19);
  finish_state(st);
  if (n < 8) {
#pragma unroll
    for (int i = 0; i < 4; ++i) lds[L_PARTB + wv * 128 + opos(32 * g + 8 * i + n)] = st.acc[i];
  }
  if (lane == 0) { lds[L_MLB + 2 * wv] = st.m; lds[L_MLB + 2 * wv + 1] = st.l; }
  __syncthreads();
  {
    float M = -INFINITY;
#pragma unroll
    for (int k = 0; k < 8; ++k) M = fmaxf(M, lds[L_MLB + 2 * k]);
    float L = 0.f;
    f32x4 o4 = {0.f, 0.f, 0.f, 0.f};
#pragma unroll
    for (int k = 0; k < 8; ++k) {
      const float f = __builtin_amdgcn_exp2f((lds[L_MLB + 2 * k] - M) * C2);
      L = fmaf(f, lds[L_MLB + 2 * k + 1], L);
      o4 += f * *(const f32x4*)&lds[L_PARTB + k * 128 + 16 * wv + 4 * g];
    }
    if (tid == 0)
      __builtin_amdgcn_raw_buffer_store_b128(v4u{__float_as_uint(M), tag, __float_as_uint(L), tag}, rWS, WS_MLB + (hB * 8 + sB) * 128, 0, AUX_SC1);
    f32x4 Y[4];
#pragma unroll
    for (int cb = 0; cb < 4; ++cb) Y[cb] = f32x4{0.f, 0.f, 0.f, 0.f};
#pragma unroll
    for (int rg = 0; rg < 4; ++rg) {
      const v4u fr = andm(split_rep(o4[rg], n >= 8), mask);
#pragma unroll
      for (int cb = 0; cb < 4; ++cb) Y[cb] = mfma(wob[rg * 4 + cb], fr, Y[cb]);
    }
#pragma unroll
    for (int cb = 0; cb < 4; ++cb) {
#pragma unroll
      for (int i = 0; i < 4; ++i) Y[cb][i] += dppf<ROR8>(Y[cb][i]);
      if (n < 8) *(f32x4*)&lds[L_YPART + wv * 512 + ((cb * 4 + g) * 8 + n) * 4] = Y[cb];
    }
  }
  __syncthreads();
  STAMP(20);
  if (tid < 256) {
    const int c = 2 * tid;
    const int idx = (((c >> 7) * 4 + ((c >> 5) & 3)) * 8 + (c & 7)) * 4 + ((c >> 3) & 3);
    float y0 = 0.f, y1 = 0.f;
#pragma unroll
    for (int k = 0; k < 8; ++k) { y0 += lds[L_YPART + k * 512 + idx]; y1 += lds[L_YPART + k * 512 + idx + 4]; }
    __builtin_amdgcn_raw_buffer_store_b128(v4u{__float_as_uint(y0), tag, __float_as_uint(y1), tag}, rWS,
                                           WS_YB + (hB * 8 + sB) * 4096 + tid * 16, 0, AUX_SC1);
  }
  STAMP(21);
  {
    const bool isB = wv < 4;
    const unsigned yoff = (isB ? WS_YB : WS_YA) + (tid & 255) * 4096 + w * 16;
    v4u y = ld128<AUX_SC1>(rWS, yoff, 0), ml = v4u{0u, tag, 0u, tag};
    if (!TAIL2 && isB) ml = ld128<AUX_SC1>(rWS, WS_MLB + tid * 128, 0);
    for (unsigned spins = 0; spins < SPIN_CAP; ++spins) {
      const bool ok = tag2(y, tag) && tag2(ml, tag);
      if (__all(ok) || ((ABL & 1) && !isB)) break;
      __builtin_amdgcn_s_sleep(POLL_SLEEP);
      MEMFENCE();
      if (!ok) {
        y = ld128<AUX_SC1>(rWS, yoff, 0);
        if (!TAIL2 && isB) ml = ld128<AUX_SC1>(rWS, WS_MLB + tid * 128, 0);
      }
    }
    STAMP(22);
    float v0 = __uint_as_float(y.x), v1 = __uint_as_float(y.z);
    if (!TAIL2 && isB) {
      const float m = __uint_as_float(ml.x);
      float M = fmaxf(m, dppf<XOR1>(m));
      M = fmaxf(M, dppf<XOR2>(M));
      M = fmaxf(M, dppf<HMIRROR>(M));
      const float f = __builtin_amdgcn_exp2f((m - M) * C2);
      float L = f * __uint_as_float(ml.z);
      L += dppf<XOR1>(L); L += dppf<XOR2>(L); L += dppf<HMIRROR>(L);
      const float c = f / L;
      v0 *= c; v1 *= c;
    }
    v0 += dppf<XOR1>(v0); v0 += dppf<XOR2>(v0); v0 += dppf<ROR4>(v0); v0 += dppf<ROR8>(v0); v0 = xrow_sum(v0);
    v1 += dppf<XOR1>(v1); v1 += dppf<XOR2>(v1); v1 += dppf<ROR4>(v1); v1 += dppf<ROR8>(v1); v1 = xrow_sum(v1);
    if (lane == 0) { lds[L_RED + 2 * wv] = v0; lds[L_RED + 2 * wv + 1] = v1; }
  }
  __syncthreads();
  if (lane == 0 && (wv == 0 || wv == 4)) {
    const float* r = &lds[L_RED + 2 * wv];
    const float s0 = (r[0] + r[2]) + (r[4] + r[6]), s1 = (r[1] + r[3]) + (r[5] + r[7]);
    *(unsigned*)(out + (wv == 0 ? 512 : 0) + 2 * w) = cvtpk(s0, s1);
    if (w == 0 && wv == 0) __builtin_amdgcn_raw_buffer_store_b32(tag, rWS, WS_GEN, 0, AUX_SC1);
  }
}

}

extern "C" void kernel_launch(void* const* d_in, const int* in_sizes, int n_in,
                              void* d_out, int out_size, void* d_ws, size_t ws_size,
                              hipStream_t stream) {
  (void)in_sizes; (void)n_in; (void)out_size; (void)ws_size;
  fused_decode<<<256, 512, 0, stream>>>(
      (const uint16_t*)d_in[0], (const uint16_t*)d_in[1], (const uint16_t*)d_in[2], (const uint16_t*)d_in[3], (const uint16_t*)d_in[4],
      (const uint16_t*)d_in[5], (const uint16_t*)d_in[6], (const uint16_t*)d_in[7], (const uint16_t*)d_in[8], (const uint16_t*)d_in[9],
      (uint16_t*)d_out, (unsigned char*)d_ws);
}
```

```cpp
#include <hip/hip_runtime.h>
#include <hip/hip_bf16.h>
#include <stdint.h>
#include <utility>

#define GAS __attribute__((address_space(1)))
typedef unsigned v4u __attribute__((ext_vector_type(4)));
typedef unsigned v2u __attribute__((ext_vector_type(2)));
typedef float f32x4 __attribute__((ext_vector_type(4)));
typedef float f32x2 __attribute__((ext_vector_type(2)));
typedef short bf16x8 __attribute__((ext_vector_type(8)));
typedef __bf16 bf16x2_t __attribute__((ext_vector_type(2)));
typedef __amdgpu_buffer_rsrc_t rsrc_t;

namespace {

#define DIST 4
#define LOCKSTEP 2
#define POLL_SLEEP 4
#define K_AUX 2
#define V_AUX 2
#define WQ_AUX 2
#define WO_AUX 2
#define END_SPIN 0

constexpr int AUX_NT = 2, AUX_SC1 = 16;
constexpr int RSRC_W3 = 0x00020000;
constexpr float SCALE = 0.08838834764831845f;
constexpr float C2 = 1.4426950408889634f;
constexpr int SK = 16384, WIN0 = 16384 - 4096;
constexpr int NSLOT = 17;
constexpr int PRE = 5;
constexpr int next_unit(int u) { return (u + DIST >= PRE && u + DIST < 2 * NSLOT) ? u + DIST : -1; }
constexpr int NBUF = (DIST + 1) / 2;
constexpr int S_QA_G1 = 0, S_QA_R = S_QA_G1 + NBUF;
constexpr int S_QA_G2 = 5, S_QA_L = S_QA_G2 + NBUF;
constexpr int SA = S_QA_L + 1;
constexpr int S_PA_C = SA + 2;
constexpr int S_PA_G = 12, S_PA_R = S_PA_G + NBUF;
constexpr int S_WO = 5, S_WOA = 13;
constexpr int S_YA = S_PA_R + 1;
static_assert(S_YA < NSLOT && S_PA_C < S_PA_G, "schedule");
constexpr unsigned SPIN_CAP = 1u << 18;

constexpr unsigned WS_GEN = 0;
constexpr unsigned WS_PQB = 4096;
constexpr unsigned WS_PQA = WS_PQB + 256 * 512;
constexpr unsigned WS_QA = WS_PQA + 256 * 512;
constexpr unsigned PA_REC = 640;
constexpr unsigned WS_PA = WS_QA + 8 * 32 * 128;
constexpr unsigned WS_MLB = WS_PA + 8 * 32 * 4 * PA_REC;
constexpr unsigned WS_YA = WS_MLB + 256 * 128;
constexpr unsigned WS_YB = WS_YA + 256 * 2048;
constexpr unsigned WS_END = WS_YB + 256 * 2048;

constexpr int L_QPART = 0;
constexpr int L_QSUM = L_QPART + 2 * 8 * 128;
constexpr int L_PARTA = L_QSUM + 8 * 128;
constexpr int L_PARTB = L_PARTA + 8 * 4 * 132;
constexpr int L_MLB = L_PARTB + 8 * 128;
constexpr int L_YPART = L_MLB + 16;
constexpr int L_RED = L_YPART + 8 * 512;
constexpr int L_OFIN = L_RED + 16;
constexpr int L_QA = L_OFIN + 16;
constexpr int L_FLAG = L_QA + 256;
constexpr int L_END = L_FLAG + 4;

__device__ __forceinline__ rsrc_t mk_rsrc(const void* p, unsigned bytes) {
  return __builtin_amdgcn_make_buffer_rsrc(const_cast<void*>(p), 0, bytes, RSRC_W3);
}
template <int AUX> __device__ __forceinline__ v4u ld128(rsrc_t rs, unsigned voff, unsigned soff) {
  return __builtin_amdgcn_raw_buffer_load_b128(rs, voff, soff, AUX);
}
__device__ __forceinline__ f32x4 mfma(v4u a, v4u b, f32x4 c) {
  return __builtin_amdgcn_mfma_f32_16x16x32_bf16(__builtin_bit_cast(bf16x8, a), __builtin_bit_cast(bf16x8, b), c, 0, 0, 0);
}
__device__ __forceinline__ unsigned cvtpk(float lo, float hi) {
  f32x2 v = {lo, hi};
  return __builtin_bit_cast(unsigned, __builtin_convertvector(v, bf16x2_t));
}
template <int CTRL> __device__ __forceinline__ float dppf(float x) {
  return __builtin_bit_cast(float, __builtin_amdgcn_mov_dpp(__builtin_bit_cast(int, x), CTRL, 0xf, 0xf, true));
}
template <int CTRL, int RM, int BM> __device__ __forceinline__ unsigned dppu(unsigned old, unsigned x) {
  return (unsigned)__builtin_amdgcn_update_dpp((int)old, (int)x, CTRL, RM, BM, false);
}
constexpr int XOR1 = 0xB1, XOR2 = 0x4E, HMIRROR = 0x141, ROR8 = 0x128, ROR4 = 0x124, BCAST = 0x150;
__device__ __forceinline__ float x16_sum(float x) {
  auto s = __builtin_amdgcn_permlane16_swap(__float_as_uint(x), __float_as_uint(x), false, false);
  return __uint_as_float(s[0]) + __uint_as_float(s[1]);
}
__device__ __forceinline__ float x32_sum(float x) {
  auto t = __builtin_amdgcn_permlane32_swap(__float_as_uint(x), __float_as_uint(x), false, false);
  return __uint_as_float(t[0]) + __uint_as_float(t[1]);
}
__device__ __forceinline__ float xrow_max(float x) {
  auto s = __builtin_amdgcn_permlane16_swap(__float_as_uint(x), __float_as_uint(x), false, false);
  x = fmaxf(__uint_as_float(s[0]), __uint_as_float(s[1]));
  auto t = __builtin_amdgcn_permlane32_swap(__float_as_uint(x), __float_as_uint(x), false, false);
  return fmaxf(__uint_as_float(t[0]), __uint_as_float(t[1]));
}
__device__ __forceinline__ float xrow_sum(float x) { return x32_sum(x16_sum(x)); }
__device__ __forceinline__ v4u andm(unsigned rep, const v4u& m) { return v4u{rep & m.x, rep & m.y, rep & m.z, rep & m.w}; }
__device__ __forceinline__ unsigned split_rep(float x, bool take_lo) {
  const unsigned hi = cvtpk(x, x);
  const float r = x - __uint_as_float(hi & 0xffff0000u);
  const unsigned lo = cvtpk(r, r);
  return take_lo ? lo : hi;
}
__device__ __forceinline__ unsigned tb(float v, unsigned byte) { return ((__float_as_uint(v) + 0x80u) & 0xffffff00u) | byte; }
__device__ __forceinline__ v4u pack4(float a, float b, float c, float d, unsigned tag) {
  return v4u{tb(a, tag & 0xffu), tb(b, (tag >> 8) & 0xffu), tb(c, (tag >> 16) & 0xffu), tb(d, tag >> 24)};
}
__device__ __forceinline__ bool ok4(const v4u& x, unsigned tag) {
  return (((x.x ^ tag) | (x.y ^ (tag >> 8)) | (x.z ^ (tag >> 16)) | (x.w ^ (tag >> 24))) & 0xffu) == 0u;
}
__device__ __forceinline__ f32x4 un4(const v4u& x) {
  return f32x4{__uint_as_float(x.x & 0xffffff00u), __uint_as_float(x.y & 0xffffff00u), __uint_as_float(x.z & 0xffffff00u),
               __uint_as_float(x.w & 0xffffff00u)};
}
__device__ __forceinline__ bool tag2(const v4u& x, unsigned tag) { return x.y == tag && x.w == tag; }

#define STAMP(k)
#define SB() __builtin_amdgcn_sched_barrier(0)
#define MEMFENCE() asm volatile("" ::: "memory")
#define PIN(x) asm volatile("" : "+v"(x) :: "memory")
#define AWAIT(OK, RELOAD) do { if (__builtin_expect(!(OK), 0)) { unsigned spins_ = 0; \
    do { __builtin_amdgcn_s_sleep(2); MEMFENCE(); RELOAD; } while (!(OK) && ++spins_ < SPIN_CAP); } } while (0)
#define LDS_FLAG(i) __hip_atomic_load((unsigned*)&lds[L_FLAG + (i)], __ATOMIC_RELAXED, __HIP_MEMORY_SCOPE_WORKGROUP)

template <typename F, int... I>
__device__ __forceinline__ void run_slots(F& f, std::integer_sequence<int, I...>) { (f(std::integral_constant<int, I>{}), ...); }

struct State { float m, l; f32x4 acc; };

__device__ __forceinline__ void qk_part(const v4u (&K)[4], const v4u (&Bq)[4], f32x4& S) {
  S = f32x4{0.f, 0.f, 0.f, 0.f};
#pragma unroll
  for (int u = 0; u < 4; ++u) S = mfma(K[u], Bq[u], S);
}
__device__ __forceinline__ void sm_pv(const f32x4& S, const v4u (&V)[4], const v4u& mask, int n, State& st) {
  const float s01 = (n & 1) ? S[1] : S[0], s23 = (n & 1) ? S[3] : S[2];
  float s = (n & 2) ? s23 : s01;
  s += dppf<XOR1>(s);
  s += dppf<XOR2>(s);
  s = __uint_as_float(cvtpk(s, s) & 0xffff0000u) * SCALE;
  s = __uint_as_float(cvtpk(s, s) & 0xffff0000u);
  float tm = fmaxf(s, dppf<ROR4>(s));
  tm = fmaxf(tm, dppf<ROR8>(tm));
  tm = xrow_max(tm);
  const float mn = fmaxf(st.m, tm);
  const float alpha = __builtin_amdgcn_exp2f((st.m - mn) * C2);
  const float p = __builtin_amdgcn_exp2f((s - mn) * C2);
  st.m = mn;
  st.l = fmaf(st.l, alpha, p);
  st.acc *= alpha;
  const unsigned pk = split_rep(p, (n & 2) != 0);
  unsigned rep;
#define PV_STEP(u) rep = dppu<BCAST + 4 * (u), 0xf, 0x3>(pk, pk); rep = dppu<BCAST + 4 * (u) + 2, 0xf, 0xc>(rep, pk); \
  st.acc = mfma(V[u], andm(rep, mask), st.acc);
  PV_STEP(0) PV_STEP(1) PV_STEP(2) PV_STEP(3)
#undef PV_STEP
}
__device__ __forceinline__ void finish_state(State& st) {
  float l = st.l;
  l += dppf<ROR4>(l);
  l += dppf<ROR8>(l);
  st.l = xrow_sum(l);
#pragma unroll
  for (int i = 0; i < 4; ++i) st.acc[i] += dppf<ROR8>(st.acc[i]);
}
__device__ __forceinline__ int opos(int d) { return (d >> 4) * 16 + (d & 3) * 4 + ((d >> 2) & 3); }

__global__ void __launch_bounds__(512, 2)
fused_decode(const uint16_t* __restrict__ xA, const uint16_t* __restrict__ kA, const uint16_t* __restrict__ vA,
             const uint16_t* __restrict__ wqA, const uint16_t* __restrict__ woA,
             const uint16_t* __restrict__ xB, const uint16_t* __restrict__ kB, const uint16_t* __restrict__ vB,
             const uint16_t* __restrict__ wqB, const uint16_t* __restrict__ woB,
             uint16_t* __restrict__ out, unsigned char* ws) {
  __shared__ __attribute__((aligned(16))) float lds[L_END];
  const int tid = threadIdx.x, lane = tid & 63, wv = __builtin_amdgcn_readfirstlane(tid >> 6);
  const int g = lane >> 4, n = lane & 15;
  STAMP(0);
  const int w = blockIdx.x, x8 = w & 7, j = w >> 3;
  const int hB = 4 * x8 + (j & 3), sB = j >> 2;
  const int pB = hB * 8 + sB;
  const int hAq = 4 * x8 + (j >> 3), ks = j & 7;

  const rsrc_t rKB = mk_rsrc(kB + ((size_t)hB * SK + sB * 2048) * 128, 2048 * 256);
  const rsrc_t rVB = mk_rsrc(vB + ((size_t)hB * SK + sB * 2048) * 128, 2048 * 256);
  const rsrc_t rKA = mk_rsrc(kA + ((size_t)x8 * SK + WIN0 + j * 128) * 128, 128 * 256);
  const rsrc_t rVA = mk_rsrc(vA + ((size_t)x8 * SK + WIN0 + j * 128) * 128, 128 * 256);
  const rsrc_t rWQB = mk_rsrc(wqB, 512 * 8192);
  const rsrc_t rWQA = mk_rsrc(wqA, 512 * 8192);
  const rsrc_t rWOB = mk_rsrc(woB, 4096 * 1024);
  const rsrc_t rWOA = mk_rsrc(woA, 4096 * 1024);
  const rsrc_t rXB = mk_rsrc(xB, 1024);
  const rsrc_t rXA = mk_rsrc(xA, 1024);
  const rsrc_t rWS = mk_rsrc(ws, WS_END);

  const unsigned koff = (n >> 2) * 256 + g * 64 + (n & 3) * 16, voff = lane * 16;
  const unsigned wvoff = wv * 4096;

  v4u qa, g1, g2o[2], g2m[2], woa[2], wob[16];
  v4u wqb[2], wqa[2];
  unsigned short xbr[2], xar[2];
#pragma unroll
  for (int t = 0; t < 2; ++t) {
    wqb[t] = ld128<WQ_AUX>(rWQB, g * 8192 + n * 16, (64 * sB + 8 * wv + 4 * t) * 8192 + hB * 256);
    xbr[t] = __builtin_amdgcn_raw_buffer_load_b16(rXB, g * 2, (64 * sB + 8 * wv + 4 * t) * 2, 0);
  }
#pragma unroll
  for (int t = 0; t < 2; ++t) {
    wqa[t] = ld128<WQ_AUX>(rWQA, g * 8192 + n * 16, (64 * ks + 8 * wv + 4 * t) * 8192 + hAq * 256);
    xar[t] = __builtin_amdgcn_raw_buffer_load_b16(rXA, g * 2, (64 * ks + 8 * wv + 4 * t) * 2, 0);
  }
  const unsigned gen = __builtin_amdgcn_raw_buffer_load_b32(rWS, WS_GEN, 0, AUX_SC1);
  unsigned pt[6];
  pt[0] = __builtin_amdgcn_raw_buffer_load_b32(rKA, 0, wvoff, AUX_NT);
  pt[1] = __builtin_amdgcn_raw_buffer_load_b32(rVA, 0, wvoff, AUX_NT);
  pt[2] = __builtin_amdgcn_raw_buffer_load_b32(rWOA, 0, (512 * x8 + 16 * j) * 1024, AUX_NT);
  pt[3] = __builtin_amdgcn_raw_buffer_load_b32(rWOB, 0, (hB * 128 + 16 * wv) * 1024, AUX_NT);
  pt[4] = __builtin_amdgcn_raw_buffer_load_b32(rWS, 0, WS_YA + w * 2048, AUX_SC1);
  pt[5] = __builtin_amdgcn_raw_buffer_load_b32(rWS, 0, WS_YB + w * 2048, AUX_SC1);
  SB();
  v4u K[NSLOT][4], V[NSLOT][4];
  auto issue = [&](int u) __attribute__((always_inline)) {
    const int s = u >> 1;
    const bool isA = s == SA;
    const int tb_ = s < SA ? s : s - 1;
    if (u & 1) {
#pragma unroll
      for (int k = 0; k < 4; ++k) V[s][k] = ld128<V_AUX>(isA ? rVA : rVB, voff + k * 1024, wvoff + (isA ? 0 : tb_ * 32768));
    } else {
#pragma unroll
      for (int k = 0; k < 4; ++k) K[s][k] = ld128<K_AUX>(isA ? rKA : rKB, koff + k * 1024, wvoff + (isA ? 0 : tb_ * 32768));
    }
  };
  if (tid < 4) lds[L_FLAG + tid] = 0.f;
  __builtin_amdgcn_s_barrier();
  issue(0);
  SB();
  const unsigned tag = __builtin_amdgcn_readfirstlane(gen) + 1u;
  v4u mask;
  {
    const unsigned hm = (n & 1) ? 0xffff0000u : 0x0000ffffu;
    const int q = (n & 7) >> 1;
    mask = v4u{q == 0 ? hm : 0u, q == 1 ? hm : 0u, q == 2 ? hm : 0u, q == 3 ? hm : 0u};
  }
  {
    f32x4 DB = {0.f, 0.f, 0.f, 0.f}, DA = {0.f, 0.f, 0.f, 0.f};
#pragma unroll
    for (int t = 0; t < 2; ++t) {
      DB = mfma(wqb[t], andm((unsigned)xbr[t] * 0x10001u, mask), DB);
      DA = mfma(wqa[t], andm((unsigned)xar[t] * 0x10001u, mask), DA);
    }
    if (n < 8) {
      *(f32x4*)&lds[L_QPART + wv * 128 + (g * 8 + n) * 4] = DB;
      *(f32x4*)&lds[L_QPART + 1024 + wv * 128 + (g * 8 + n) * 4] = DA;
    }
  }
  STAMP(24);
  __syncthreads();
  if (wv < 2) {
    const int u = lane & 31, c = 4 * u, hf = lane >> 5;
    const int e = ((c >> 5) * 8 + (c & 7)) * 4 + ((c >> 3) & 3);
    float y[4] = {0.f, 0.f, 0.f, 0.f};
#pragma unroll
    for (int k = 0; k < 4; ++k)
#pragma unroll
      for (int i = 0; i < 4; ++i) y[i] += lds[L_QPART + wv * 1024 + (4 * hf + k) * 128 + e + 4 * i];
#pragma unroll
    for (int i = 0; i < 4; ++i) y[i] = x32_sum(y[i]);
    const unsigned dst = wv == 0 ? WS_PQB + pB * 512 : WS_PQA + (hAq * 8 + ks) * 512;
    if (lane < 32) __builtin_amdgcn_raw_buffer_store_b128(pack4(y[0], y[1], y[2], y[3], tag), rWS, dst + u * 16, 0, AUX_SC1);
  }
  STAMP(25);
  SB();
  issue(1);
  SB();
  PIN(K[0][3]);
  issue(2);
  SB();
  PIN(V[0][3]);
  STAMP(26);
  {
    const unsigned src = WS_PQB + (hB * 8 + wv) * 512 + (lane & 31) * 16;
    v4u pq = ld128<AUX_SC1>(rWS, src, 0);
    SB();
    issue(3);
    SB();
    PIN(K[1][3]);
    STAMP(28);
    issue(4);
    SB();
    AWAIT(__all(ok4(pq, tag)), pq = ld128<AUX_SC1>(rWS, src, 0));
    if (lane < 32) *(f32x4*)&lds[L_QSUM + wv * 128 + 4 * lane] = un4(pq);
  }
  asm volatile("" :: "v"(pt[0]), "v"(pt[1]), "v"(pt[2]), "v"(pt[3]), "v"(pt[4]), "v"(pt[5]));
  STAMP(27);
  __syncthreads();
  v4u Bq[4];
  {
    f32x4 a = {0.f, 0.f, 0.f, 0.f}, b = {0.f, 0.f, 0.f, 0.f};
#pragma unroll
    for (int k = 0; k < 8; ++k) {
      a += *(const f32x4*)&lds[L_QSUM + k * 128 + 8 * (4 * g + (n & 3))];
      b += *(const f32x4*)&lds[L_QSUM + k * 128 + 8 * (4 * g + (n & 3)) + 4];
    }
    const v4u qB = v4u{cvtpk(a[0], a[1]), cvtpk(a[2], a[3]), cvtpk(b[0], b[1]), cvtpk(b[2], b[3])};
#pragma unroll
    for (int u = 0; u < 4; ++u) Bq[u] = ((n >> 2) == u) ? qB : v4u{0u, 0u, 0u, 0u};
  }
  STAMP(1);
  State st;
  st.m = -INFINITY; st.l = 0.f; st.acc = f32x4{0.f, 0.f, 0.f, 0.f};

  auto outproj64 = [&](const float* of, const v4u (&wr)[2], unsigned ydst) __attribute__((always_inline)) {
    f32x4 Y = {0.f, 0.f, 0.f, 0.f};
#pragma unroll
    for (int t = 0; t < 2; ++t) {
      const float o = of[8 * t + 2 * g + (n >> 3)];
      Y = mfma(wr[t], andm(split_rep(o, false), mask), Y);
      Y = mfma(wr[t], andm(split_rep(o, true), mask), Y);
    }
    const bool valid = (g >> 1) == (n >> 3);
    float z[4];
#pragma unroll
    for (int i = 0; i < 4; ++i) {
      float t = valid ? Y[i] : 0.f;
      t += dppf<ROR8>(t);
      z[i] = x32_sum(t);
    }
    const int i = 2 * (g >> 1) + (n >> 3);
    const float zi = (i & 2) ? ((i & 1) ? z[3] : z[2]) : ((i & 1) ? z[1] : z[0]);
    const float z1 = dppf<0x55>(zi), z2 = dppf<0xAA>(zi), z3 = dppf<0xFF>(zi);
    if (!(n & 3))
      __builtin_amdgcn_raw_buffer_store_b128(pack4(zi, z1, z2, z3, tag), rWS, ydst + (64 * wv + 32 * (g & 1) + 8 * i + (n & 7)) * 4, 0, AUX_SC1);
  };
  const unsigned g2_base = WS_PA + ((x8 * 32 + (lane >> 2)) * 4 + (j >> 3)) * PA_REC;
  auto g2_load = [&]() __attribute__((always_inline)) {
#pragma unroll
    for (int k = 0; k < 2; ++k) {
      g2m[k] = ld128<AUX_SC1>(rWS, g2_base + k * 16 * 4 * PA_REC, 0);
      g2o[k] = ld128<AUX_SC1>(rWS, g2_base + k * 16 * 4 * PA_REC + 16 + (4 * (j & 7) + (lane & 3)) * 16, 0);
    }
  };
  auto g2_ok = [&]() __attribute__((always_inline)) {
    return (bool)__all(ok4(g2m[0], tag) && ok4(g2m[1], tag) && ok4(g2o[0], tag) && ok4(g2o[1], tag));
  };
  auto oa_merge = [&]() __attribute__((always_inline)) {
    const f32x4 ml0 = un4(g2m[0]), ml1 = un4(g2m[1]);
    float M = fmaxf(ml0[0], ml1[0]);
    M = fmaxf(M, dppf<ROR4>(M));
    M = fmaxf(M, dppf<ROR8>(M));
    M = xrow_max(M);
    const float f0 = __builtin_amdgcn_exp2f((ml0[0] - M) * C2), f1 = __builtin_amdgcn_exp2f((ml1[0] - M) * C2);
    float L = f0 * ml0[1] + f1 * ml1[1];
    f32x4 a = f0 * un4(g2o[0]) + f1 * un4(g2o[1]);
    L += dppf<ROR4>(L); L += dppf<ROR8>(L); L = xrow_sum(L);
#pragma unroll
    for (int i = 0; i < 4; ++i) { a[i] += dppf<ROR4>(a[i]); a[i] += dppf<ROR8>(a[i]); a[i] = xrow_sum(a[i]); }
    if (lane < 4) *(f32x4*)&lds[L_OFIN + 4 * lane] = a * (1.f / L);
    asm volatile("s_waitcnt lgkmcnt(0)" ::: "memory");
    if (lane == 0) __hip_atomic_store((unsigned*)&lds[L_FLAG + 1], 1u, __ATOMIC_RELAXED, __HIP_MEMORY_SCOPE_WORKGROUP);
  };
  bool oa_done = false, ya_done = false;

  auto slot = [&](auto S_) __attribute__((always_inline)) {
    constexpr int s = decltype(S_)::value;
    if constexpr (s != SA) {
      f32x4 S;
      qk_part(K[s], Bq, S);
      PIN(S);
      SB();
      if constexpr (next_unit(2 * s) >= 0) issue(next_unit(2 * s));
      SB();
      sm_pv(S, V[s], mask, n, st);
      PIN(st.acc); PIN(st.l);
      SB();
      if constexpr (next_unit(2 * s + 1) >= 0) issue(next_unit(2 * s + 1));
      SB();
    } else {
      issue(next_unit(2 * s)); issue(next_unit(2 * s + 1));
      SB();
      while (LDS_FLAG(2) != 2u) __builtin_amdgcn_s_sleep(1);
      MEMFENCE();
      State sa[4];
#pragma unroll
      for (int hd = 0; hd < 4; ++hd) {
        const v4u q = *(const v4u*)&lds[L_QA + hd * 64 + (4 * g + (n & 3)) * 4];
        v4u Ba[4];
#pragma unroll
        for (int u = 0; u < 4; ++u) Ba[u] = ((n >> 2) == u) ? q : v4u{0u, 0u, 0u, 0u};
        sa[hd].m = -INFINITY; sa[hd].l = 0.f; sa[hd].acc = f32x4{0.f, 0.f, 0.f, 0.f};
        f32x4 S;
        qk_part(K[s], Ba, S);
        sm_pv(S, V[s], mask, n, sa[hd]);
        finish_state(sa[hd]);
      }
#pragma unroll
      for (int hd = 0; hd < 4; ++hd) {
        float* rec = &lds[L_PARTA + (wv * 4 + hd) * 132];
        if (n < 8) {
#pragma unroll
          for (int i = 0; i < 4; ++i) rec[4 + 32 * g + 8 * i + n] = sa[hd].acc[i];
        }
        if (lane == 0) { rec[0] = sa[hd].m; rec[1] = sa[hd].l; }
      }
      asm volatile("s_waitcnt lgkmcnt(0)" ::: "memory");
      if (lane == 0) __hip_atomic_fetch_add((unsigned*)&lds[L_FLAG], 1u, __ATOMIC_RELAXED, __HIP_MEMORY_SCOPE_WORKGROUP);
      SB();
    }
    if constexpr (s == S_PA_C) {
      while (LDS_FLAG(0) != 8u) __builtin_amdgcn_s_sleep(1);
      MEMFENCE();
      const int hd = wv >> 1, u = 16 * (wv & 1) + n;
      float M = -INFINITY;
#pragma unroll
      for (int k = 0; k < 8; ++k) M = fmaxf(M, lds[L_PARTA + (k * 4 + hd) * 132]);
      const float* r0 = &lds[L_PARTA + (2 * g * 4 + hd) * 132];
      const float* r1 = r0 + 4 * 132;
      const float f0 = __builtin_amdgcn_exp2f((r0[0] - M) * C2), f1 = __builtin_amdgcn_exp2f((r1[0] - M) * C2);
      f32x4 o = f0 * *(const f32x4*)&r0[4 + 4 * u] + f1 * *(const f32x4*)&r1[4 + 4 * u];
      const float L = xrow_sum(f0 * r0[1] + f1 * r1[1]);
#pragma unroll
      for (int i = 0; i < 4; ++i) o[i] = xrow_sum(o[i]);
      const unsigned base = WS_PA + ((x8 * 32 + j) * 4 + hd) * PA_REC;
      if (lane < 16) __builtin_amdgcn_raw_buffer_store_b128(pack4(o[0], o[1], o[2], o[3], tag), rWS, base + 16 + u * 16, 0, AUX_SC1);
      if (lane == 16 && !(wv & 1)) __builtin_amdgcn_raw_buffer_store_b128(pack4(M, L, 0.f, 0.f, tag), rWS, base, 0, AUX_SC1);
      SB();
    }
    STAMP(2 + s);
    if constexpr (LOCKSTEP && s % (LOCKSTEP ? LOCKSTEP : 1) == LOCKSTEP - 1 && s + 1 < NSLOT) { SB(); __builtin_amdgcn_s_barrier(); SB(); }
    if constexpr (s == S_QA_G1) if (wv == 4) {
      g1 = ld128<AUX_SC1>(rWS, WS_PQA + (hAq * 8 + ((lane >> 2) & 7)) * 512 + (4 * ks + (lane & 3)) * 16, 0);
      SB();
    }
    if constexpr (s == S_QA_R) if (wv == 4) {
      AWAIT(__all(ok4(g1, tag)), g1 = ld128<AUX_SC1>(rWS, WS_PQA + (hAq * 8 + ((lane >> 2) & 7)) * 512 + (4 * ks + (lane & 3)) * 16, 0));
      f32x4 v = un4(g1);
#pragma unroll
      for (int i = 0; i < 4; ++i) { v[i] += dppf<ROR4>(v[i]); v[i] += dppf<ROR8>(v[i]); v[i] = x16_sum(v[i]); }
      if (lane < 4)
        __builtin_amdgcn_raw_buffer_store_b128(v4u{cvtpk(v[0], v[1]), tag, cvtpk(v[2], v[3]), tag}, rWS, WS_QA + (x8 * 32 + j) * 128 + lane * 16, 0, AUX_SC1);
      SB();
    }
    if constexpr (s == S_QA_G2) if (wv < 2) {
      qa = ld128<AUX_SC1>(rWS, WS_QA + (x8 * 32 + 16 * wv + (lane >> 2)) * 128 + (lane & 3) * 16, 0);
      SB();
    }
    if constexpr (s == S_QA_L) if (wv < 2) {
      AWAIT(__all(tag2(qa, tag)), qa = ld128<AUX_SC1>(rWS, WS_QA + (x8 * 32 + 16 * wv + (lane >> 2)) * 128 + (lane & 3) * 16, 0));
      *(v2u*)&lds[L_QA + 2 * (64 * wv + lane)] = v2u{qa.x, qa.z};
      asm volatile("s_waitcnt lgkmcnt(0)" ::: "memory");
      if (lane == 0) __hip_atomic_fetch_add((unsigned*)&lds[L_FLAG + 2], 1u, __ATOMIC_RELAXED, __HIP_MEMORY_SCOPE_WORKGROUP);
      SB();
    }
    if constexpr (s == S_PA_G) if (wv == 7) { g2_load(); SB(); }
    if constexpr (s == S_PA_R) if (wv == 7) {
      if (g2_ok()) { oa_merge(); oa_done = true; }
      SB();
    }
    if constexpr (s == S_YA) {
      if (LDS_FLAG(1) == 1u) {
        MEMFENCE();
        outproj64(&lds[L_OFIN], woa, WS_YA + w * 2048);
        ya_done = true;
      }
      SB();
    }
    if constexpr (s >= S_WO && s < S_WO + 8) {
#pragma unroll
      for (int k = 2 * (s - S_WO); k < 2 * (s - S_WO) + 2; ++k)
        wob[k] = ld128<WO_AUX>(rWOB, g * 1024 + n * 16, (hB * 128 + 16 * wv + 4 * (k >> 2)) * 1024 + (k & 3) * 256);
      SB();
    }
    if constexpr (s == S_WOA) {
#pragma unroll
      for (int t = 0; t < 2; ++t)
        woa[t] = ld128<AUX_NT>(rWOA, (lane >> 3) * 1024 + (lane & 7) * 16, (512 * x8 + 16 * j + 8 * t) * 1024 + wv * 128);
      SB();
    }
  };
  run_slots(slot, std::make_integer_sequence<int, NSLOT>{});

  STAMP(19);
  finish_state(st);
  if (n < 8) {
#pragma unroll
    for (int i = 0; i < 4; ++i) lds[L_PARTB + wv * 128 + opos(32 * g + 8 * i + n)] = st.acc[i];
  }
  if (lane == 0) { lds[L_MLB + 2 * wv] = st.m; lds[L_MLB + 2 * wv + 1] = st.l; }
  __syncthreads();
  {
    float M = -INFINITY;
#pragma unroll
    for (int k = 0; k < 8; ++k) M = fmaxf(M, lds[L_MLB + 2 * k]);
    float L = 0.f;
    f32x4 o4 = {0.f, 0.f, 0.f, 0.f};
#pragma unroll
    for (int k = 0; k < 8; ++k) {
      const float f = __builtin_amdgcn_exp2f((lds[L_MLB + 2 * k] - M) * C2);
      L = fmaf(f, lds[L_MLB + 2 * k + 1], L);
      o4 += f * *(const f32x4*)&lds[L_PARTB + k * 128 + 16 * wv + 4 * g];
    }
    if (tid == 0) __builtin_amdgcn_raw_buffer_store_b128(pack4(M, L, 0.f, 0.f, tag), rWS, WS_MLB + pB * 128, 0, AUX_SC1);
    f32x4 Y[4];
#pragma unroll
    for (int cb = 0; cb < 4; ++cb) Y[cb] = f32x4{0.f, 0.f, 0.f, 0.f};
#pragma unroll
    for (int rg = 0; rg < 4; ++rg) {
      const v4u fr = andm(split_rep(o4[rg], n >= 8), mask);
#pragma unroll
      for (int cb = 0; cb < 4; ++cb) Y[cb] = mfma(wob[rg * 4 + cb], fr, Y[cb]);
    }
#pragma unroll
    for (int cb = 0; cb < 4; ++cb) {
#pragma unroll
      for (int i = 0; i < 4; ++i) Y[cb][i] += dppf<ROR8>(Y[cb][i]);
      if (n < 8) *(f32x4*)&lds[L_YPART + wv * 512 + ((cb * 4 + g) * 8 + n) * 4] = Y[cb];
    }
  }
  __syncthreads();
  STAMP(20);
  if (tid < 256) {
    const int u = (wv << 5) | (lane & 31), c = 4 * u, hf = lane >> 5;
    const int idx = (((c >> 7) * 4 + ((c >> 5) & 3)) * 8 + (c & 7)) * 4 + ((c >> 3) & 3);
    float y[4] = {0.f, 0.f, 0.f, 0.f};
#pragma unroll
    for (int k = 0; k < 4; ++k)
#pragma unroll
      for (int i = 0; i < 4; ++i) y[i] += lds[L_YPART + (4 * hf + k) * 512 + idx + 4 * i];
#pragma unroll
    for (int i = 0; i < 4; ++i) y[i] = x32_sum(y[i]);
    if (lane < 32) __builtin_amdgcn_raw_buffer_store_b128(pack4(y[0], y[1], y[2], y[3], tag), rWS, WS_YB + pB * 2048 + u * 16, 0, AUX_SC1);
  }
  {
    if (wv == 7 && !oa_done) {
      MEMFENCE();
      g2_load();
      AWAIT(g2_ok(), g2_load());
      oa_merge();
    }
    if (!ya_done) {
      while (LDS_FLAG(1) != 1u) __builtin_amdgcn_s_sleep(1);
      MEMFENCE();
      outproj64(&lds[L_OFIN], woa, WS_YA + w * 2048);
    }
  }
  STAMP(21);
  if (wv < 4) {
    const bool isB = w >= 128;
    const unsigned yoff = (isB ? WS_YB : WS_YA) + tid * 2048 + (w & 127) * 16;
    v4u y = ld128<AUX_SC1>(rWS, yoff, 0), ml = pack4(0.f, 1.f, 0.f, 0.f, tag);
    if (isB) ml = ld128<AUX_SC1>(rWS, WS_MLB + tid * 128, 0);
    for (unsigned spins = 0; spins < SPIN_CAP; ++spins) {
      const bool ok = ok4(y, tag) && ok4(ml, tag);
      if (__all(ok)) break;
      __builtin_amdgcn_s_sleep(POLL_SLEEP);
      MEMFENCE();
      if (!ok) {
        y = ld128<AUX_SC1>(rWS, yoff, 0);
        if (isB) ml = ld128<AUX_SC1>(rWS, WS_MLB + tid * 128, 0);
      }
    }
    STAMP(22);
    f32x4 v = un4(y);
    if (isB) {
      const f32x4 mlf = un4(ml);
      const float m = mlf[0];
      float M = fmaxf(m, dppf<XOR1>(m));
      M = fmaxf(M, dppf<XOR2>(M));
      M = fmaxf(M, dppf<HMIRROR>(M));
      const float f = __builtin_amdgcn_exp2f((m - M) * C2);
      float L = f * mlf[1];
      L += dppf<XOR1>(L); L += dppf<XOR2>(L); L += dppf<HMIRROR>(L);
      v *= f / L;
    }
#pragma unroll
    for (int i = 0; i < 4; ++i) {
      float t = v[i];
      t += dppf<XOR1>(t); t += dppf<XOR2>(t); t += dppf<ROR4>(t); t += dppf<ROR8>(t);
      v[i] = xrow_sum(t);
    }
    if (lane == 0) *(f32x4*)&lds[L_RED + 4 * wv] = v;
  }
  __syncthreads();
  if (tid == 0) {
    const f32x4* r = (const f32x4*)&lds[L_RED];
    const f32x4 s = (r[0] + r[1]) + (r[2] + r[3]);
    *(v2u*)(out + (w >= 128 ? 512 : 0) + 4 * (w & 127)) = v2u{cvtpk(s[0], s[1]), cvtpk(s[2], s[3])};
    if (w == 128) __builtin_amdgcn_raw_buffer_store_b32(tag, rWS, WS_GEN, 0, AUX_SC1);
  }
}

}

extern "C" void kernel_launch(void* const* d_in, const int* in_sizes, int n_in,
                              void* d_out, int out_size, void* d_ws, size_t ws_size,
                              hipStream_t stream) {
  (void)in_sizes; (void)n_in; (void)out_size; (void)ws_size;
  fused_decode<<<256, 512, 0, stream>>>(
      (const uint16_t*)d_in[0], (const uint16_t*)d_in[1], (const uint16_t*)d_in[2], (const uint16_t*)d_in[3], (const uint16_t*)d_in[4],
      (const uint16_t*)d_in[5], (const uint16_t*)d_in[6], (const uint16_t*)d_in[7], (const uint16_t*)d_in[8], (const uint16_t*)d_in[9],
      (uint16_t*)d_out, (unsigned char*)d_ws);
}
```

```cpp
#define LOCKSTEP 1
#include <hip/hip_runtime.h>
#include <hip/hip_bf16.h>
#include <stdint.h>
#include <utility>

#define GAS __attribute__((address_space(1)))
typedef unsigned v4u __attribute__((ext_vector_type(4)));
typedef unsigned v2u __attribute__((ext_vector_type(2)));
typedef float f32x4 __attribute__((ext_vector_type(4)));
typedef float f32x2 __attribute__((ext_vector_type(2)));
typedef short bf16x8 __attribute__((ext_vector_type(8)));
typedef __bf16 bf16x2_t __attribute__((ext_vector_type(2)));
typedef __amdgpu_buffer_rsrc_t rsrc_t;

namespace {

#define DIST 4
#define POLL_SLEEP 4
#define K_AUX 2
#define V_AUX 2
#define WQ_AUX 2
#define WO_AUX 2
#define S_WO_ 1
#define WO_PER 2
#define S_WOA_ 13
#define YA_DEFER 1
#define END_SPIN 0

constexpr int AUX_NT = 2, AUX_SC1 = 16;
constexpr int RSRC_W3 = 0x00020000;
constexpr float SCALE = 0.08838834764831845f;
constexpr float C2 = 1.4426950408889634f;
constexpr int SK = 16384, WIN0 = 16384 - 4096;
constexpr int NSLOT = 17;
constexpr int PRE = 5;
constexpr int next_unit(int u) { return (u + DIST >= PRE && u + DIST < 2 * NSLOT) ? u + DIST : -1; }
constexpr int NBUF = (DIST + 1) / 2;
constexpr int S_QA_G1 = 0, S_QA_R = S_QA_G1 + NBUF;
constexpr int S_QA_G2 = 5, S_QA_L = S_QA_G2 + NBUF;
constexpr int SA = S_QA_L + 1;
constexpr int S_PA_C = SA + 2;
constexpr int S_PA_G = 12, S_PA_R = S_PA_G + NBUF;
constexpr int S_WO = S_WO_, S_WOA = S_WOA_;
constexpr int S_YA = S_PA_R + 1;
static_assert(S_YA < NSLOT && S_PA_C < S_PA_G, "schedule");
constexpr unsigned SPIN_CAP = 1u << 18;

constexpr unsigned WS_GEN = 0;
constexpr unsigned WS_PQB = 4096;
constexpr unsigned WS_PQA = WS_PQB + 256 * 512;
constexpr unsigned WS_QA = WS_PQA + 256 * 512;
constexpr unsigned PA_REC = 640;
constexpr unsigned WS_PA = WS_QA + 8 * 32 * 128;
constexpr unsigned WS_MLB = WS_PA + 8 * 32 * 4 * PA_REC;
constexpr unsigned WS_YA = WS_MLB + 256 * 128;
constexpr unsigned WS_YB = WS_YA + 256 * 2048;
constexpr unsigned WS_END = WS_YB + 256 * 2048;

constexpr int L_QPART = 0;
constexpr int L_QSUM = L_QPART + 2 * 8 * 128;
constexpr int L_PARTA = L_QSUM + 8 * 128;
constexpr int L_PARTB = L_PARTA + 8 * 4 * 132;
constexpr int L_MLB = L_PARTB + 8 * 128;
constexpr int L_YPART = L_MLB + 16;
constexpr int L_RED = L_YPART + 8 * 512;
constexpr int L_OFIN = L_RED + 16;
constexpr int L_QA = L_OFIN + 16;
constexpr int L_FLAG = L_QA + 256;
constexpr int L_END = L_FLAG + 4;

__device__ __forceinline__ rsrc_t mk_rsrc(const void* p, unsigned bytes) {
  return __builtin_amdgcn_make_buffer_rsrc(const_cast<void*>(p), 0, bytes, RSRC_W3);
}
template <int AUX> __device__ __forceinline__ v4u ld128(rsrc_t rs, unsigned voff, unsigned soff) {
  return __builtin_amdgcn_raw_buffer_load_b128(rs, voff, soff, AUX);
}
__device__ __forceinline__ f32x4 mfma(v4u a, v4u b, f32x4 c) {
  return __builtin_amdgcn_mfma_f32_16x16x32_bf16(__builtin_bit_cast(bf16x8, a), __builtin_bit_cast(bf16x8, b), c, 0, 0, 0);
}
__device__ __forceinline__ unsigned cvtpk(float lo, float hi) {
  f32x2 v = {lo, hi};
  return __builtin_bit_cast(unsigned, __builtin_convertvector(v, bf16x2_t));
}
template <int CTRL> __device__ __forceinline__ float dppf(float x) {
  return __builtin_bit_cast(float, __builtin_amdgcn_mov_dpp(__builtin_bit_cast(int, x), CTRL, 0xf, 0xf, true));
}
template <int CTRL, int RM, int BM> __device__ __forceinline__ unsigned dppu(unsigned old, unsigned x) {
  return (unsigned)__builtin_amdgcn_update_dpp((int)old, (int)x, CTRL, RM, BM, false);
}
constexpr int XOR1 = 0xB1, XOR2 = 0x4E, HMIRROR = 0x141, ROR8 = 0x128, ROR4 = 0x124, BCAST = 0x150;
__device__ __forceinline__ float x16_sum(float x) {
  auto s = __builtin_amdgcn_permlane16_swap(__float_as_uint(x), __float_as_uint(x), false, false);
  return __uint_as_float(s[0]) + __uint_as_float(s[1]);
}
__device__ __forceinline__ float x32_sum(float x) {
  auto t = __builtin_amdgcn_permlane32_swap(__float_as_uint(x), __float_as_uint(x), false, false);
  return __uint_as_float(t[0]) + __uint_as_float(t[1]);
}
__device__ __forceinline__ float xrow_max(float x) {
  auto s = __builtin_amdgcn_permlane16_swap(__float_as_uint(x), __float_as_uint(x), false, false);
  x = fmaxf(__uint_as_float(s[0]), __uint_as_float(s[1]));
  auto t = __builtin_amdgcn_permlane32_swap(__float_as_uint(x), __float_as_uint(x), false, false);
  return fmaxf(__uint_as_float(t[0]), __uint_as_float(t[1]));
}
__device__ __forceinline__ float xrow_sum(float x) { return x32_sum(x16_sum(x)); }
__device__ __forceinline__ v4u andm(unsigned rep, const v4u& m) { return v4u{rep & m.x, rep & m.y, rep & m.z, rep & m.w}; }
__device__ __forceinline__ unsigned split_rep(float x, bool take_lo) {
  const unsigned hi = cvtpk(x, x);
  const float r = x - __uint_as_float(hi & 0xffff0000u);
  const unsigned lo = cvtpk(r, r);
  return take_lo ? lo : hi;
}
__device__ __forceinline__ unsigned tb(float v, unsigned byte) { return ((__float_as_uint(v) + 0x80u) & 0xffffff00u) | byte; }
__device__ __forceinline__ v4u pack4(float a, float b, float c, float d, unsigned tag) {
  return v4u{tb(a, tag & 0xffu), tb(b, (tag >> 8) & 0xffu), tb(c, (tag >> 16) & 0xffu), tb(d, tag >> 24)};
}
__device__ __forceinline__ bool ok4(const v4u& x, unsigned tag) {
  return (((x.x ^ tag) | (x.y ^ (tag >> 8)) | (x.z ^ (tag >> 16)) | (x.w ^ (tag >> 24))) & 0xffu) == 0u;
}
__device__ __forceinline__ f32x4 un4(const v4u& x) {
  return f32x4{__uint_as_float(x.x & 0xffffff00u), __uint_as_float(x.y & 0xffffff00u), __uint_as_float(x.z & 0xffffff00u),
               __uint_as_float(x.w & 0xffffff00u)};
}
__device__ __forceinline__ bool tag2(const v4u& x, unsigned tag) { return x.y == tag && x.w == tag; }

#define STAMP(k)
#define SB() __builtin_amdgcn_sched_barrier(0)
#define MEMFENCE() asm volatile("" ::: "memory")
#define PIN(x) asm volatile("" : "+v"(x) :: "memory")
#define AWAIT(OK, RELOAD) do { if (__builtin_expect(!(OK), 0)) { unsigned spins_ = 0; \
    do { __builtin_amdgcn_s_sleep(2); MEMFENCE(); RELOAD; } while (!(OK) && ++spins_ < SPIN_CAP); } } while (0)
#define LDS_FLAG(i) __hip_atomic_load((unsigned*)&lds[L_FLAG + (i)], __ATOMIC_RELAXED, __HIP_MEMORY_SCOPE_WORKGROUP)

template <typename F, int... I>
__device__ __forceinline__ void run_slots(F& f, std::integer_sequence<int, I...>) { (f(std::integral_constant<int, I>{}), ...); }

struct State { float m, l; f32x4 acc; };

__device__ __forceinline__ void qk_part(const v4u (&K)[4], const v4u (&Bq)[4], f32x4& S) {
  S = f32x4{0.f, 0.f, 0.f, 0.f};
#pragma unroll
  for (int u = 0; u < 4; ++u) S = mfma(K[u], Bq[u], S);
}
__device__ __forceinline__ void sm_pv(const f32x4& S, const v4u (&V)[4], const v4u& mask, int n, State& st) {
  const float s01 = (n & 1) ? S[1] : S[0], s23 = (n & 1) ? S[3] : S[2];
  float s = (n & 2) ? s23 : s01;
  s += dppf<XOR1>(s);
  s += dppf<XOR2>(s);
  s = __uint_as_float(cvtpk(s, s) & 0xffff0000u) * SCALE;
  s = __uint_as_float(cvtpk(s, s) & 0xffff0000u);
  float tm = fmaxf(s, dppf<ROR4>(s));
  tm = fmaxf(tm, dppf<ROR8>(tm));
  tm = xrow_max(tm);
  const float mn = fmaxf(st.m, tm);
  const float alpha = __builtin_amdgcn_exp2f((st.m - mn) * C2);
  const float p = __builtin_amdgcn_exp2f((s - mn) * C2);
  st.m = mn;
  st.l = fmaf(st.l, alpha, p);
  st.acc *= alpha;
  const unsigned pk = split_rep(p, (n & 2) != 0);
  unsigned rep;
#define PV_STEP(u) rep = dppu<BCAST + 4 * (u), 0xf, 0x3>(pk, pk); rep = dppu<BCAST + 4 * (u) + 2, 0xf, 0xc>(rep, pk); \
  st.acc = mfma(V[u], andm(rep, mask), st.acc);
  PV_STEP(0) PV_STEP(1) PV_STEP(2) PV_STEP(3)
#undef PV_STEP
}
__device__ __forceinline__ void finish_state(State& st) {
  float l = st.l;
  l += dppf<ROR4>(l);
  l += dppf<ROR8>(l);
  st.l = xrow_sum(l);
#pragma unroll
  for (int i = 0; i < 4; ++i) st.acc[i] += dppf<ROR8>(st.acc[i]);
}
__device__ __forceinline__ int opos(int d) { return (d >> 4) * 16 + (d & 3) * 4 + ((d >> 2) & 3); }

__global__ void __launch_bounds__(512, 2)
fused_decode(const uint16_t* __restrict__ xA, const uint16_t* __restrict__ kA, const uint16_t* __restrict__ vA,
             const uint16_t* __restrict__ wqA, const uint16_t* __restrict__ woA,
             const uint16_t* __restrict__ xB, const uint16_t* __restrict__ kB, const uint16_t* __restrict__ vB,
             const uint16_t* __restrict__ wqB, const uint16_t* __restrict__ woB,
             uint16_t* __restrict__ out, unsigned char* ws) {
  __shared__ __attribute__((aligned(16))) float lds[L_END];
  const int tid = threadIdx.x, lane = tid & 63, wv = __builtin_amdgcn_readfirstlane(tid >> 6);
  const int g = lane >> 4, n = lane & 15;
  STAMP(0);
  const int w = blockIdx.x, x8 = w & 7, j = w >> 3;
  const int hB = 4 * x8 + (j & 3), sB = j >> 2;
  const int pB = hB * 8 + sB;
  const int hAq = 4 * x8 + (j >> 3), ks = j & 7;

  const rsrc_t rKB = mk_rsrc(kB + ((size_t)hB * SK + sB * 2048) * 128, 2048 * 256);
  const rsrc_t rVB = mk_rsrc(vB + ((size_t)hB * SK + sB * 2048) * 128, 2048 * 256);
  const rsrc_t rKA = mk_rsrc(kA + ((size_t)x8 * SK + WIN0 + j * 128) * 128, 128 * 256);
  const rsrc_t rVA = mk_rsrc(vA + ((size_t)x8 * SK + WIN0 + j * 128) * 128, 128 * 256);
  const rsrc_t rWQB = mk_rsrc(wqB, 512 * 8192);
  const rsrc_t rWQA = mk_rsrc(wqA, 512 * 8192);
  const rsrc_t rWOB = mk_rsrc(woB, 4096 * 1024);
  const rsrc_t rWOA = mk_rsrc(woA, 4096 * 1024);
  const rsrc_t rXB = mk_rsrc(xB, 1024);
  const rsrc_t rXA = mk_rsrc(xA, 1024);
  const rsrc_t rWS = mk_rsrc(ws, WS_END);

  const unsigned koff = (n >> 2) * 256 + g * 64 + (n & 3) * 16, voff = lane * 16;
  const unsigned wvoff = wv * 4096;

  v4u qa, g1, g2o[2], g2m[2], woa[2], wob[16];
  v4u wqb[2], wqa[2];
  unsigned short xbr[2], xar[2];
#pragma unroll
  for (int t = 0; t < 2; ++t) {
    wqb[t] = ld128<WQ_AUX>(rWQB, g * 8192 + n * 16, (64 * sB + 8 * wv + 4 * t) * 8192 + hB * 256);
    xbr[t] = __builtin_amdgcn_raw_buffer_load_b16(rXB, g * 2, (64 * sB + 8 * wv + 4 * t) * 2, 0);
  }
#pragma unroll
  for (int t = 0; t < 2; ++t) {
    wqa[t] = ld128<WQ_AUX>(rWQA, g * 8192 + n * 16, (64 * ks + 8 * wv + 4 * t) * 8192 + hAq * 256);
    xar[t] = __builtin_amdgcn_raw_buffer_load_b16(rXA, g * 2, (64 * ks + 8 * wv + 4 * t) * 2, 0);
  }
  const unsigned gen = __builtin_amdgcn_raw_buffer_load_b32(rWS, WS_GEN, 0, AUX_SC1);
  unsigned pt[6];
  pt[0] = __builtin_amdgcn_raw_buffer_load_b32(rKA, 0, wvoff, AUX_NT);
  pt[1] = __builtin_amdgcn_raw_buffer_load_b32(rVA, 0, wvoff, AUX_NT);
  pt[2] = __builtin_amdgcn_raw_buffer_load_b32(rWOA, 0, (512 * x8 + 16 * j) * 1024, AUX_NT);
  pt[3] = __builtin_amdgcn_raw_buffer_load_b32(rWOB, 0, (hB * 128 + 16 * wv) * 1024, AUX_NT);
  pt[4] = __builtin_amdgcn_raw_buffer_load_b32(rWS, 0, WS_YA + w * 2048, AUX_SC1);
  pt[5] = __builtin_amdgcn_raw_buffer_load_b32(rWS, 0, WS_YB + w * 2048, AUX_SC1);
  SB();
  v4u K[NSLOT][4], V[NSLOT][4];
  auto issue = [&](int u) __attribute__((always_inline)) {
    const int s = u >> 1;
    const bool isA = s == SA;
    const int tb_ = s < SA ? s : s - 1;
    if (u & 1) {
#pragma unroll
      for (int k = 0; k < 4; ++k) V[s][k] = ld128<V_AUX>(isA ? rVA : rVB, voff + k * 1024, wvoff + (isA ? 0 : tb_ * 32768));
    } else {
#pragma unroll
      for (int k = 0; k < 4; ++k) K[s][k] = ld128<K_AUX>(isA ? rKA : rKB, koff + k * 1024, wvoff + (isA ? 0 : tb_ * 32768));
    }
  };
  if (tid < 4) lds[L_FLAG + tid] = 0.f;
  __builtin_amdgcn_s_barrier();
  issue(0);
  SB();
  const unsigned tag = __builtin_amdgcn_readfirstlane(gen) + 1u;
  const unsigned tg4 = (tag & 0xffu) | (((tag + 85u) & 0xffu) << 8) | (((tag + 170u) & 0xffu) << 16) | (((tag + 37u) & 0xffu) << 24);
  v4u mask;
  {
    const unsigned hm = (n & 1) ? 0xffff0000u : 0x0000ffffu;
    const int q = (n & 7) >> 1;
    mask = v4u{q == 0 ? hm : 0u, q == 1 ? hm : 0u, q == 2 ? hm : 0u, q == 3 ? hm : 0u};
  }
  {
    f32x4 DB = {0.f, 0.f, 0.f, 0.f}, DA = {0.f, 0.f, 0.f, 0.f};
#pragma unroll
    for (int t = 0; t < 2; ++t) {
      DB = mfma(wqb[t], andm((unsigned)xbr[t] * 0x10001u, mask), DB);
      DA = mfma(wqa[t], andm((unsigned)xar[t] * 0x10001u, mask), DA);
    }
    if (n < 8) {
      *(f32x4*)&lds[L_QPART + wv * 128 + (g * 8 + n) * 4] = DB;
      *(f32x4*)&lds[L_QPART + 1024 + wv * 128 + (g * 8 + n) * 4] = DA;
    }
  }
  STAMP(24);
  __syncthreads();
  if (wv < 2) {
    const int u = lane & 31, c = 4 * u, hf = lane >> 5;
    const int e = ((c >> 5) * 8 + (c & 7)) * 4 + ((c >> 3) & 3);
    float y[4] = {0.f, 0.f, 0.f, 0.f};
#pragma unroll
    for (int k = 0; k < 4; ++k)
#pragma unroll
      for (int i = 0; i < 4; ++i) y[i] += lds[L_QPART + wv * 1024 + (4 * hf + k) * 128 + e + 4 * i];
#pragma unroll
    for (int i = 0; i < 4; ++i) y[i] = x32_sum(y[i]);
    const unsigned dst = wv == 0 ? WS_PQB + pB * 512 : WS_PQA + (hAq * 8 + ks) * 512;
    if (lane < 32) __builtin_amdgcn_raw_buffer_store_b128(pack4(y[0], y[1], y[2], y[3], tg4), rWS, dst + u * 16, 0, AUX_SC1);
  }
  STAMP(25);
  SB();
  issue(1);
  SB();
  PIN(K[0][3]);
  issue(2);
  SB();
  PIN(V[0][3]);
  STAMP(26);
  {
    const unsigned src = WS_PQB + (hB * 8 + wv) * 512 + (lane & 31) * 16;
    v4u pq = ld128<AUX_SC1>(rWS, src, 0);
    SB();
    issue(3);
    SB();
    PIN(K[1][3]);
    STAMP(28);
    issue(4);
    SB();
    AWAIT(__all(ok4(pq, tg4)), pq = ld128<AUX_SC1>(rWS, src, 0));
    if (lane < 32) *(f32x4*)&lds[L_QSUM + wv * 128 + 4 * lane] = un4(pq);
  }
  asm volatile("" :: "v"(pt[0]), "v"(pt[1]), "v"(pt[2]), "v"(pt[3]), "v"(pt[4]), "v"(pt[5]));
  STAMP(27);
  __syncthreads();
  v4u Bq[4];
  {
    f32x4 a = {0.f, 0.f, 0.f, 0.f}, b = {0.f, 0.f, 0.f, 0.f};
#pragma unroll
    for (int k = 0; k < 8; ++k) {
      a += *(const f32x4*)&lds[L_QSUM + k * 128 + 8 * (4 * g + (n & 3))];
      b += *(const f32x4*)&lds[L_QSUM + k * 128 + 8 * (4 * g + (n & 3)) + 4];
    }
    const v4u qB = v4u{cvtpk(a[0], a[1]), cvtpk(a[2], a[3]), cvtpk(b[0], b[1]), cvtpk(b[2], b[3])};
#pragma unroll
    for (int u = 0; u < 4; ++u) Bq[u] = ((n >> 2) == u) ? qB : v4u{0u, 0u, 0u, 0u};
  }
  STAMP(1);
  State st;
  st.m = -INFINITY; st.l = 0.f; st.acc = f32x4{0.f, 0.f, 0.f, 0.f};

  v4u ya_unit;
  auto outproj64 = [&](const float* of, const v4u (&wr)[2]) __attribute__((always_inline)) {
    f32x4 Y = {0.f, 0.f, 0.f, 0.f};
#pragma unroll
    for (int t = 0; t < 2; ++t) {
      const float o = of[8 * t + 2 * g + (n >> 3)];
      Y = mfma(wr[t], andm(split_rep(o, false), mask), Y);
      Y = mfma(wr[t], andm(split_rep(o, true), mask), Y);
    }
    const bool valid = (g >> 1) == (n >> 3);
    float z[4];
#pragma unroll
    for (int i = 0; i < 4; ++i) {
      float t = valid ? Y[i] : 0.f;
      t += dppf<ROR8>(t);
      z[i] = x32_sum(t);
    }
    const int i = 2 * (g >> 1) + (n >> 3);
    const float zi = (i & 2) ? ((i & 1) ? z[3] : z[2]) : ((i & 1) ? z[1] : z[0]);
    const float z1 = dppf<0x55>(zi), z2 = dppf<0xAA>(zi), z3 = dppf<0xFF>(zi);
    ya_unit = pack4(zi, z1, z2, z3, tg4);
  };
  const unsigned g2_base = WS_PA + ((x8 * 32 + (lane >> 2)) * 4 + (j >> 3)) * PA_REC;
  auto g2_load = [&]() __attribute__((always_inline)) {
#pragma unroll
    for (int k = 0; k < 2; ++k) {
      g2m[k] = ld128<AUX_SC1>(rWS, g2_base + k * 16 * 4 * PA_REC, 0);
      g2o[k] = ld128<AUX_SC1>(rWS, g2_base + k * 16 * 4 * PA_REC + 16 + (4 * (j & 7) + (lane & 3)) * 16, 0);
    }
  };
  auto g2_ok = [&]() __attribute__((always_inline)) {
    return (bool)__all(ok4(g2m[0], tg4) && ok4(g2m[1], tg4) && ok4(g2o[0], tg4) && ok4(g2o[1], tg4));
  };
  auto oa_merge = [&]() __attribute__((always_inline)) {
    const f32x4 ml0 = un4(g2m[0]), ml1 = un4(g2m[1]);
    float M = fmaxf(ml0[0], ml1[0]);
    M = fmaxf(M, dppf<ROR4>(M));
    M = fmaxf(M, dppf<ROR8>(M));
    M = xrow_max(M);
    const float f0 = __builtin_amdgcn_exp2f((ml0[0] - M) * C2), f1 = __builtin_amdgcn_exp2f((ml1[0] - M) * C2);
    float L = f0 * ml0[1] + f1 * ml1[1];
    f32x4 a = f0 * un4(g2o[0]) + f1 * un4(g2o[1]);
    L += dppf<ROR4>(L); L += dppf<ROR8>(L); L = xrow_sum(L);
#pragma unroll
    for (int i = 0; i < 4; ++i) { a[i] += dppf<ROR4>(a[i]); a[i] += dppf<ROR8>(a[i]); a[i] = xrow_sum(a[i]); }
    if (lane < 4) *(f32x4*)&lds[L_OFIN + 4 * lane] = a * (1.f / L);
    asm volatile("s_waitcnt lgkmcnt(0)" ::: "memory");
    if (lane == 0) __hip_atomic_store((unsigned*)&lds[L_FLAG + 1], 1u, __ATOMIC_RELAXED, __HIP_MEMORY_SCOPE_WORKGROUP);
  };
  auto ya_store = [&]() __attribute__((always_inline)) {
    if (!(n & 3))
      __builtin_amdgcn_raw_buffer_store_b128(ya_unit, rWS, WS_YA + w * 2048 + (64 * wv + 32 * (g & 1) + 16 * (g >> 1) + 8 * (n >> 3) + (n & 7)) * 4, 0, AUX_SC1);
  };
  bool oa_done = false, ya_done = false;

  auto slot = [&](auto S_) __attribute__((always_inline)) {
    constexpr int s = decltype(S_)::value;
    if constexpr (s != SA) {
      f32x4 S;
      qk_part(K[s], Bq, S);
      PIN(S);
      SB();
      if constexpr (next_unit(2 * s) >= 0) issue(next_unit(2 * s));
      SB();
      sm_pv(S, V[s], mask, n, st);
      PIN(st.acc); PIN(st.l);
      SB();
      if constexpr (next_unit(2 * s + 1) >= 0) issue(next_unit(2 * s + 1));
      SB();
    } else {
      issue(next_unit(2 * s)); issue(next_unit(2 * s + 1));
      SB();
      while (LDS_FLAG(2) != 2u) __builtin_amdgcn_s_sleep(1);
      MEMFENCE();
      State sa[4];
#pragma unroll
      for (int hd = 0; hd < 4; ++hd) {
        const v4u q = *(const v4u*)&lds[L_QA + hd * 64 + (4 * g + (n & 3)) * 4];
        v4u Ba[4];
#pragma unroll
        for (int u = 0; u < 4; ++u) Ba[u] = ((n >> 2) == u) ? q : v4u{0u, 0u, 0u, 0u};
        sa[hd].m = -INFINITY; sa[hd].l = 0.f; sa[hd].acc = f32x4{0.f, 0.f, 0.f, 0.f};
        f32x4 S;
        qk_part(K[s], Ba, S);
        sm_pv(S, V[s], mask, n, sa[hd]);
        finish_state(sa[hd]);
      }
#pragma unroll
      for (int hd = 0; hd < 4; ++hd) {
        float* rec = &lds[L_PARTA + (wv * 4 + hd) * 132];
        if (n < 8) {
#pragma unroll
          for (int i = 0; i < 4; ++i) rec[4 + 32 * g + 8 * i + n] = sa[hd].acc[i];
        }
        if (lane == 0) { rec[0] = sa[hd].m; rec[1] = sa[hd].l; }
      }
      asm volatile("s_waitcnt lgkmcnt(0)" ::: "memory");
      if (lane == 0) __hip_atomic_fetch_add((unsigned*)&lds[L_FLAG], 1u, __ATOMIC_RELAXED, __HIP_MEMORY_SCOPE_WORKGROUP);
      SB();
    }
    if constexpr (s == S_PA_C) {
      while (LDS_FLAG(0) != 8u) __builtin_amdgcn_s_sleep(1);
      MEMFENCE();
      const int hd = wv >> 1, u = 16 * (wv & 1) + n;
      float M = -INFINITY;
#pragma unroll
      for (int k = 0; k < 8; ++k) M = fmaxf(M, lds[L_PARTA + (k * 4 + hd) * 132]);
      const float* r0 = &lds[L_PARTA + (2 * g * 4 + hd) * 132];
      const float* r1 = r0 + 4 * 132;
      const float f0 = __builtin_amdgcn_exp2f((r0[0] - M) * C2), f1 = __builtin_amdgcn_exp2f((r1[0] - M) * C2);
      f32x4 o = f0 * *(const f32x4*)&r0[4 + 4 * u] + f1 * *(const f32x4*)&r1[4 + 4 * u];
      const float L = xrow_sum(f0 * r0[1] + f1 * r1[1]);
#pragma unroll
      for (int i = 0; i < 4; ++i) o[i] = xrow_sum(o[i]);
      const unsigned base = WS_PA + ((x8 * 32 + j) * 4 + hd) * PA_REC;
      if (lane < 16) __builtin_amdgcn_raw_buffer_store_b128(pack4(o[0], o[1], o[2], o[3], tg4), rWS, base + 16 + u * 16, 0, AUX_SC1);
      if (lane == 16 && !(wv & 1)) __builtin_amdgcn_raw_buffer_store_b128(pack4(M, L, 0.f, 0.f, tg4), rWS, base, 0, AUX_SC1);
      SB();
    }
    STAMP(2 + s);
    if constexpr (LOCKSTEP && s % (LOCKSTEP ? LOCKSTEP : 1) == LOCKSTEP - 1 && s + 1 < NSLOT) { SB(); __builtin_amdgcn_s_barrier(); SB(); }
    if constexpr (s == S_QA_G1) if (wv == 4) {
      g1 = ld128<AUX_SC1>(rWS, WS_PQA + (hAq * 8 + ((lane >> 2) & 7)) * 512 + (4 * ks + (lane & 3)) * 16, 0);
      SB();
    }
    if constexpr (s == S_QA_R) if (wv == 4) {
      AWAIT(__all(ok4(g1, tg4)), g1 = ld128<AUX_SC1>(rWS, WS_PQA + (hAq * 8 + ((lane >> 2) & 7)) * 512 + (4 * ks + (lane & 3)) * 16, 0));
      f32x4 v = un4(g1);
#pragma unroll
      for (int i = 0; i < 4; ++i) { v[i] += dppf<ROR4>(v[i]); v[i] += dppf<ROR8>(v[i]); v[i] = x16_sum(v[i]); }
      if (lane < 4)
        __builtin_amdgcn_raw_buffer_store_b128(v4u{cvtpk(v[0], v[1]), tag, cvtpk(v[2], v[3]), tag}, rWS, WS_QA + (x8 * 32 + j) * 128 + lane * 16, 0, AUX_SC1);
      SB();
    }
    if constexpr (s == S_QA_G2) if (wv < 2) {
      qa = ld128<AUX_SC1>(rWS, WS_QA + (x8 * 32 + 16 * wv + (lane >> 2)) * 128 + (lane & 3) * 16, 0);
      SB();
    }
    if constexpr (s == S_QA_L) if (wv < 2) {
      AWAIT(__all(tag2(qa, tag)), qa = ld128<AUX_SC1>(rWS, WS_QA + (x8 * 32 + 16 * wv + (lane >> 2)) * 128 + (lane & 3) * 16, 0));
      *(v2u*)&lds[L_QA + 2 * (64 * wv + lane)] = v2u{qa.x, qa.z};
      asm volatile("s_waitcnt lgkmcnt(0)" ::: "memory");
      if (lane == 0) __hip_atomic_fetch_add((unsigned*)&lds[L_FLAG + 2], 1u, __ATOMIC_RELAXED, __HIP_MEMORY_SCOPE_WORKGROUP);
      SB();
    }
    if constexpr (s == S_PA_G) if (wv == 7) { g2_load(); SB(); }
    if constexpr (s == S_PA_R) if (wv == 7) {
      if (g2_ok()) { oa_merge(); oa_done = true; }
      SB();
    }
    if constexpr (s == S_YA) {
      if (LDS_FLAG(1) == 1u) {
        MEMFENCE();
        outproj64(&lds[L_OFIN], woa);
        if (!YA_DEFER) ya_store();
        ya_done = true;
      }
      SB();
    }
    if constexpr (S_WO >= 0 && s >= S_WO && s < S_WO + 16 / WO_PER) {
#pragma unroll
      for (int k = WO_PER * (s - S_WO); k < WO_PER * (s - S_WO) + WO_PER; ++k)
        wob[k] = ld128<WO_AUX>(rWOB, g * 1024 + n * 16, (hB * 128 + 16 * wv + 4 * (k >> 2)) * 1024 + (k & 3) * 256);
      SB();
    }
    if constexpr (s == S_WOA) {
#pragma unroll
      for (int t = 0; t < 2; ++t)
        woa[t] = ld128<AUX_NT>(rWOA, (lane >> 3) * 1024 + (lane & 7) * 16, (512 * x8 + 16 * j + 8 * t) * 1024 + wv * 128);
      SB();
    }
  };
  run_slots(slot, std::make_integer_sequence<int, NSLOT>{});

  STAMP(19);
  if (YA_DEFER && ya_done) ya_store();
  finish_state(st);
  if (n < 8) {
#pragma unroll
    for (int i = 0; i < 4; ++i) lds[L_PARTB + wv * 128 + opos(32 * g + 8 * i + n)] = st.acc[i];
  }
  if (lane == 0) { lds[L_MLB + 2 * wv] = st.m; lds[L_MLB + 2 * wv + 1] = st.l; }
  __syncthreads();
  {
    float M = -INFINITY;
#pragma unroll
    for (int k = 0; k < 8; ++k) M = fmaxf(M, lds[L_MLB + 2 * k]);
    float L = 0.f;
    f32x4 o4 = {0.f, 0.f, 0.f, 0.f};
#pragma unroll
    for (int k = 0; k < 8; ++k) {
      const float f = __builtin_amdgcn_exp2f((lds[L_MLB + 2 * k] - M) * C2);
      L = fmaf(f, lds[L_MLB + 2 * k + 1], L);
      o4 += f * *(const f32x4*)&lds[L_PARTB + k * 128 + 16 * wv + 4 * g];
    }
    if (tid == 0) __builtin_amdgcn_raw_buffer_store_b128(pack4(M, L, 0.f, 0.f, tg4), rWS, WS_MLB + pB * 128, 0, AUX_SC1);
    f32x4 Y[4];
#pragma unroll
    for (int cb = 0; cb < 4; ++cb) Y[cb] = f32x4{0.f, 0.f, 0.f, 0.f};
#pragma unroll
    for (int rg = 0; rg < 4; ++rg) {
      const v4u fr = andm(split_rep(o4[rg], n >= 8), mask);
#pragma unroll
      for (int cb = 0; cb < 4; ++cb) Y[cb] = mfma(wob[rg * 4 + cb], fr, Y[cb]);
    }
#pragma unroll
    for (int cb = 0; cb < 4; ++cb) {
#pragma unroll
      for (int i = 0; i < 4; ++i) Y[cb][i] += dppf<ROR8>(Y[cb][i]);
      if (n < 8) *(f32x4*)&lds[L_YPART + wv * 512 + ((cb * 4 + g) * 8 + n) * 4] = Y[cb];
    }
  }
  __syncthreads();
  STAMP(20);
  if (tid < 256) {
    const int u = (wv << 5) | (lane & 31), c = 4 * u, hf = lane >> 5;
    const int idx = (((c >> 7) * 4 + ((c >> 5) & 3)) * 8 + (c & 7)) * 4 + ((c >> 3) & 3);
    float y[4] = {0.f, 0.f, 0.f, 0.f};
#pragma unroll
    for (int k = 0; k < 4; ++k)
#pragma unroll
      for (int i = 0; i < 4; ++i) y[i] += lds[L_YPART + (4 * hf + k) * 512 + idx + 4 * i];
#pragma unroll
    for (int i = 0; i < 4; ++i) y[i] = x32_sum(y[i]);
    if (lane < 32) __builtin_amdgcn_raw_buffer_store_b128(pack4(y[0], y[1], y[2], y[3], tg4), rWS, WS_YB + pB * 2048 + u * 16, 0, AUX_SC1);
  }
  {
    if (wv == 7 && !oa_done) {
      MEMFENCE();
      g2_load();
      AWAIT(g2_ok(), g2_load());
      oa_merge();
    }
    if (!ya_done) {
      while (LDS_FLAG(1) != 1u) __builtin_amdgcn_s_sleep(1);
      MEMFENCE();
      outproj64(&lds[L_OFIN], woa);
      ya_store();
    }
  }
  STAMP(21);
  if (wv < 4) {
    const bool isB = w >= 128;
    const unsigned yoff = (isB ? WS_YB : WS_YA) + tid * 2048 + (w & 127) * 16;
    v4u y = ld128<AUX_SC1>(rWS, yoff, 0), ml = pack4(0.f, 1.f, 0.f, 0.f, tg4);
    if (isB) ml = ld128<AUX_SC1>(rWS, WS_MLB + tid * 128, 0);
    for (unsigned spins = 0; spins < SPIN_CAP; ++spins) {
      const bool ok = ok4(y, tg4) && ok4(ml, tg4);
      if (__all(ok)) break;
      __builtin_amdgcn_s_sleep(POLL_SLEEP);
      MEMFENCE();
      if (!ok) {
        y = ld128<AUX_SC1>(rWS, yoff, 0);
        if (isB) ml = ld128<AUX_SC1>(rWS, WS_MLB + tid * 128, 0);
      }
    }
    STAMP(22);
    f32x4 v = un4(y);
    if (isB) {
      const f32x4 mlf = un4(ml);
      const float m = mlf[0];
      float M = fmaxf(m, dppf<XOR1>(m));
      M = fmaxf(M, dppf<XOR2>(M));
      M = fmaxf(M, dppf<HMIRROR>(M));
      const float f = __builtin_amdgcn_exp2f((m - M) * C2);
      float L = f * mlf[1];
      L += dppf<XOR1>(L); L += dppf<XOR2>(L); L += dppf<HMIRROR>(L);
      v *= f / L;
    }
#pragma unroll
    for (int i = 0; i < 4; ++i) {
      float t = v[i];
      t += dppf<XOR1>(t); t += dppf<XOR2>(t); t += dppf<ROR4>(t); t += dppf<ROR8>(t);
      v[i] = xrow_sum(t);
    }
    if (lane == 0) *(f32x4*)&lds[L_RED + 4 * wv] = v;
  }
  __syncthreads();
  if (tid == 0) {
    const f32x4* r = (const f32x4*)&lds[L_RED];
    const f32x4 s = (r[0] + r[1]) + (r[2] + r[3]);
    *(v2u*)(out + (w >= 128 ? 512 : 0) + 4 * (w & 127)) = v2u{cvtpk(s[0], s[1]), cvtpk(s[2], s[3])};
    if (w == 128) __builtin_amdgcn_raw_buffer_store_b32(tag, rWS, WS_GEN, 0, AUX_SC1);
  }
}

}

extern "C" void kernel_launch(void* const* d_in, const int* in_sizes, int n_in,
                              void* d_out, int out_size, void* d_ws, size_t ws_size,
                              hipStream_t stream) {
  (void)in_sizes; (void)n_in; (void)out_size; (void)ws_size;
  fused_decode<<<256, 512, 0, stream>>>(
      (const uint16_t*)d_in[0], (const uint16_t*)d_in[1], (const uint16_t*)d_in[2], (const uint16_t*)d_in[3], (const uint16_t*)d_in[4],
      (const uint16_t*)d_in[5], (const uint16_t*)d_in[6], (const uint16_t*)d_in[7], (const uint16_t*)d_in[8], (const uint16_t*)d_in[9],
      (uint16_t*)d_out, (unsigned char*)d_ws);
}
```

```cpp
#define ROT_EXPR (w>>3)
#include <hip/hip_runtime.h>
#include <hip/hip_bf16.h>
#include <stdint.h>
#include <utility>

#define GAS __attribute__((address_space(1)))
typedef unsigned v4u __attribute__((ext_vector_type(4)));
typedef unsigned v2u __attribute__((ext_vector_type(2)));
typedef float f32x4 __attribute__((ext_vector_type(4)));
typedef float f32x2 __attribute__((ext_vector_type(2)));
typedef short bf16x8 __attribute__((ext_vector_type(8)));
typedef __bf16 bf16x2_t __attribute__((ext_vector_type(2)));
typedef __amdgpu_buffer_rsrc_t rsrc_t;

namespace {

#define DIST 4
#define LOCKSTEP 2
#define POLL_SLEEP 4
#define K_AUX 2
#define V_AUX 2
#define WQ_AUX 2
#define WO_AUX 2
#define S_WO_ 1
#define WO_PER 2
#define S_WOA_ 13
#define YA_DEFER 1
#define END_SPIN 0

constexpr int AUX_NT = 2, AUX_SC1 = 16;
constexpr int RSRC_W3 = 0x00020000;
constexpr float SCALE = 0.08838834764831845f;
constexpr float C2 = 1.4426950408889634f;
constexpr int SK = 16384, WIN0 = 16384 - 4096;
constexpr int NSLOT = 17;
constexpr int PRE = 5;
constexpr int next_unit(int u) { return (u + DIST >= PRE && u + DIST < 2 * NSLOT) ? u + DIST : -1; }
constexpr int NBUF = (DIST + 1) / 2;
constexpr int S_QA_G1 = 0, S_QA_R = S_QA_G1 + NBUF;
constexpr int S_QA_G2 = 5, S_QA_L = S_QA_G2 + NBUF;
constexpr int SA = S_QA_L + 1;
constexpr int S_PA_C = SA + 2;
constexpr int S_PA_G = 12, S_PA_R = S_PA_G + NBUF;
constexpr int S_WO = S_WO_, S_WOA = S_WOA_;
constexpr int S_YA = S_PA_R + 1;
static_assert(S_YA < NSLOT && S_PA_C < S_PA_G, "schedule");
constexpr unsigned SPIN_CAP = 1u << 18;

constexpr unsigned WS_GEN = 0;
constexpr unsigned WS_PQB = 4096;
constexpr unsigned WS_PQA = WS_PQB + 256 * 512;
constexpr unsigned WS_QA = WS_PQA + 256 * 512;
constexpr unsigned PA_REC = 640;
constexpr unsigned WS_PA = WS_QA + 8 * 32 * 128;
constexpr unsigned WS_MLB = WS_PA + 8 * 32 * 4 * PA_REC;
constexpr unsigned WS_YA = WS_MLB + 256 * 128;
constexpr unsigned WS_YB = WS_YA + 256 * 2048;
constexpr unsigned WS_END = WS_YB + 256 * 2048;

constexpr int L_QPART = 0;
constexpr int L_QSUM = L_QPART + 2 * 8 * 128;
constexpr int L_PARTA = L_QSUM + 8 * 128;
constexpr int L_PARTB = L_PARTA + 8 * 4 * 132;
constexpr int L_MLB = L_PARTB + 8 * 128;
constexpr int L_YPART = L_MLB + 16;
constexpr int L_RED = L_YPART + 8 * 512;
constexpr int L_OFIN = L_RED + 16;
constexpr int L_QA = L_OFIN + 16;
constexpr int L_FLAG = L_QA + 256;
constexpr int L_END = L_FLAG + 4;

__device__ __forceinline__ rsrc_t mk_rsrc(const void* p, unsigned bytes) {
  return __builtin_amdgcn_make_buffer_rsrc(const_cast<void*>(p), 0, bytes, RSRC_W3);
}
template <int AUX> __device__ __forceinline__ v4u ld128(rsrc_t rs, unsigned voff, unsigned soff) {
  return __builtin_amdgcn_raw_buffer_load_b128(rs, voff, soff, AUX);
}
__device__ __forceinline__ f32x4 mfma(v4u a, v4u b, f32x4 c) {
  return __builtin_amdgcn_mfma_f32_16x16x32_bf16(__builtin_bit_cast(bf16x8, a), __builtin_bit_cast(bf16x8, b), c, 0, 0, 0);
}
__device__ __forceinline__ unsigned cvtpk(float lo, float hi) {
  f32x2 v = {lo, hi};
  return __builtin_bit_cast(unsigned, __builtin_convertvector(v, bf16x2_t));
}
template <int CTRL> __device__ __forceinline__ float dppf(float x) {
  return __builtin_bit_cast(float, __builtin_amdgcn_mov_dpp(__builtin_bit_cast(int, x), CTRL, 0xf, 0xf, true));
}
template <int CTRL, int RM, int BM> __device__ __forceinline__ unsigned dppu(unsigned old, unsigned x) {
  return (unsigned)__builtin_amdgcn_update_dpp((int)old, (int)x, CTRL, RM, BM, false);
}
constexpr int XOR1 = 0xB1, XOR2 = 0x4E, HMIRROR = 0x141, ROR8 = 0x128, ROR4 = 0x124, BCAST = 0x150;
__device__ __forceinline__ float x16_sum(float x) {
  auto s = __builtin_amdgcn_permlane16_swap(__float_as_uint(x), __float_as_uint(x), false, false);
  return __uint_as_float(s[0]) + __uint_as_float(s[1]);
}
__device__ __forceinline__ float x32_sum(float x) {
  auto t = __builtin_amdgcn_permlane32_swap(__float_as_uint(x), __float_as_uint(x), false, false);
  return __uint_as_float(t[0]) + __uint_as_float(t[1]);
}
__device__ __forceinline__ float xrow_max(float x) {
  auto s = __builtin_amdgcn_permlane16_swap(__float_as_uint(x), __float_as_uint(x), false, false);
  x = fmaxf(__uint_as_float(s[0]), __uint_as_float(s[1]));
  auto t = __builtin_amdgcn_permlane32_swap(__float_as_uint(x), __float_as_uint(x), false, false);
  return fmaxf(__uint_as_float(t[0]), __uint_as_float(t[1]));
}
__device__ __forceinline__ float xrow_sum(float x) { return x32_sum(x16_sum(x)); }
__device__ __forceinline__ v4u andm(unsigned rep, const v4u& m) { return v4u{rep & m.x, rep & m.y, rep & m.z, rep & m.w}; }
__device__ __forceinline__ unsigned split_rep(float x, bool take_lo) {
  const unsigned hi = cvtpk(x, x);
  const float r = x - __uint_as_float(hi & 0xffff0000u);
  const unsigned lo = cvtpk(r, r);
  return take_lo ? lo : hi;
}
__device__ __forceinline__ unsigned tb(float v, unsigned byte) { return ((__float_as_uint(v) + 0x80u) & 0xffffff00u) | byte; }
__device__ __forceinline__ v4u pack4(float a, float b, float c, float d, unsigned tag) {
  return v4u{tb(a, tag & 0xffu), tb(b, (tag >> 8) & 0xffu), tb(c, (tag >> 16) & 0xffu), tb(d, tag >> 24)};
}
__device__ __forceinline__ bool ok4(const v4u& x, unsigned tag) {
  return (((x.x ^ tag) | (x.y ^ (tag >> 8)) | (x.z ^ (tag >> 16)) | (x.w ^ (tag >> 24))) & 0xffu) == 0u;
}
__device__ __forceinline__ f32x4 un4(const v4u& x) {
  return f32x4{__uint_as_float(x.x & 0xffffff00u), __uint_as_float(x.y & 0xffffff00u), __uint_as_float(x.z & 0xffffff00u),
               __uint_as_float(x.w & 0xffffff00u)};
}
__device__ __forceinline__ bool tag2(const v4u& x, unsigned tag) { return x.y == tag && x.w == tag; }

#define STAMP(k)
#define SB() __builtin_amdgcn_sched_barrier(0)
#define MEMFENCE() asm volatile("" ::: "memory")
#define PIN(x) asm volatile("" : "+v"(x) :: "memory")
#define AWAIT(OK, RELOAD) do { if (__builtin_expect(!(OK), 0)) { unsigned spins_ = 0; \
    do { __builtin_amdgcn_s_sleep(2); MEMFENCE(); RELOAD; } while (!(OK) && ++spins_ < SPIN_CAP); } } while (0)
#define LDS_FLAG(i) __hip_atomic_load((unsigned*)&lds[L_FLAG + (i)], __ATOMIC_RELAXED, __HIP_MEMORY_SCOPE_WORKGROUP)

template <typename F, int... I>
__device__ __forceinline__ void run_slots(F& f, std::integer_sequence<int, I...>) { (f(std::integral_constant<int, I>{}), ...); }

struct State { float m, l; f32x4 acc; };

__device__ __forceinline__ void qk_part(const v4u (&K)[4], const v4u (&Bq)[4], f32x4& S) {
  S = f32x4{0.f, 0.f, 0.f, 0.f};
#pragma unroll
  for (int u = 0; u < 4; ++u) S = mfma(K[u], Bq[u], S);
}
__device__ __forceinline__ void sm_pv(const f32x4& S, const v4u (&V)[4], const v4u& mask, int n, State& st) {
  const float s01 = (n & 1) ? S[1] : S[0], s23 = (n & 1) ? S[3] : S[2];
  float s = (n & 2) ? s23 : s01;
  s += dppf<XOR1>(s);
  s += dppf<XOR2>(s);
  s = __uint_as_float(cvtpk(s, s) & 0xffff0000u) * SCALE;
  s = __uint_as_float(cvtpk(s, s) & 0xffff0000u);
  float tm = fmaxf(s, dppf<ROR4>(s));
  tm = fmaxf(tm, dppf<ROR8>(tm));
  tm = xrow_max(tm);
  const float mn = fmaxf(st.m, tm);
  const float alpha = __builtin_amdgcn_exp2f((st.m - mn) * C2);
  const float p = __builtin_amdgcn_exp2f((s - mn) * C2);
  st.m = mn;
  st.l = fmaf(st.l, alpha, p);
  st.acc *= alpha;
  const unsigned pk = split_rep(p, (n & 2) != 0);
  unsigned rep;
#define PV_STEP(u) rep = dppu<BCAST + 4 * (u), 0xf, 0x3>(pk, pk); rep = dppu<BCAST + 4 * (u) + 2, 0xf, 0xc>(rep, pk); \
  st.acc = mfma(V[u], andm(rep, mask), st.acc);
  PV_STEP(0) PV_STEP(1) PV_STEP(2) PV_STEP(3)
#undef PV_STEP
}
__device__ __forceinline__ void finish_state(State& st) {
  float l = st.l;
  l += dppf<ROR4>(l);
  l += dppf<ROR8>(l);
  st.l = xrow_sum(l);
#pragma unroll
  for (int i = 0; i < 4; ++i) st.acc[i] += dppf<ROR8>(st.acc[i]);
}
__device__ __forceinline__ int opos(int d) { return (d >> 4) * 16 + (d & 3) * 4 + ((d >> 2) & 3); }

__global__ void __launch_bounds__(512, 2)
fused_decode(const uint16_t* __restrict__ xA, const uint16_t* __restrict__ kA, const uint16_t* __restrict__ vA,
             const uint16_t* __restrict__ wqA, const uint16_t* __restrict__ woA,
             const uint16_t* __restrict__ xB, const uint16_t* __restrict__ kB, const uint16_t* __restrict__ vB,
             const uint16_t* __restrict__ wqB, const uint16_t* __restrict__ woB,
             uint16_t* __restrict__ out, unsigned char* ws) {
  __shared__ __attribute__((aligned(16))) float lds[L_END];
  const int tid = threadIdx.x, lane = tid & 63, wv = __builtin_amdgcn_readfirstlane(tid >> 6);
  const int g = lane >> 4, n = lane & 15;
  STAMP(0);
  const int w = blockIdx.x, x8 = w & 7, j = w >> 3;
  const int hB = 4 * x8 + (j & 3), sB = j >> 2;
  const int pB = hB * 8 + sB;
  const int hAq = 4 * x8 + (j >> 3), ks = j & 7;

  const rsrc_t rKB = mk_rsrc(kB + ((size_t)hB * SK + sB * 2048) * 128, 2048 * 256);
  const rsrc_t rVB = mk_rsrc(vB + ((size_t)hB * SK + sB * 2048) * 128, 2048 * 256);
  const rsrc_t rKA = mk_rsrc(kA + ((size_t)x8 * SK + WIN0 + j * 128) * 128, 128 * 256);
  const rsrc_t rVA = mk_rsrc(vA + ((size_t)x8 * SK + WIN0 + j * 128) * 128, 128 * 256);
  const rsrc_t rWQB = mk_rsrc(wqB, 512 * 8192);
  const rsrc_t rWQA = mk_rsrc(wqA, 512 * 8192);
  const rsrc_t rWOB = mk_rsrc(woB, 4096 * 1024);
  const rsrc_t rWOA = mk_rsrc(woA, 4096 * 1024);
  const rsrc_t rXB = mk_rsrc(xB, 1024);
  const rsrc_t rXA = mk_rsrc(xA, 1024);
  const rsrc_t rWS = mk_rsrc(ws, WS_END);

  const unsigned koff = (n >> 2) * 256 + g * 64 + (n & 3) * 16, voff = lane * 16;
  const unsigned wvoff = wv * 4096;
  const int rot = (ROT_EXPR) & 15;

  v4u qa, g1, g2o[2], g2m[2], woa[2], wob[16];
  v4u wqb[2], wqa[2];
  unsigned short xbr[2], xar[2];
#pragma unroll
  for (int t = 0; t < 2; ++t) {
    wqb[t] = ld128<WQ_AUX>(rWQB, g * 8192 + n * 16, (64 * sB + 8 * wv + 4 * t) * 8192 + hB * 256);
    xbr[t] = __builtin_amdgcn_raw_buffer_load_b16(rXB, g * 2, (64 * sB + 8 * wv + 4 * t) * 2, 0);
  }
#pragma unroll
  for (int t = 0; t < 2; ++t) {
    wqa[t] = ld128<WQ_AUX>(rWQA, g * 8192 + n * 16, (64 * ks + 8 * wv + 4 * t) * 8192 + hAq * 256);
    xar[t] = __builtin_amdgcn_raw_buffer_load_b16(rXA, g * 2, (64 * ks + 8 * wv + 4 * t) * 2, 0);
  }
  const unsigned gen = __builtin_amdgcn_raw_buffer_load_b32(rWS, WS_GEN, 0, AUX_SC1);
  unsigned pt[6];
  pt[0] = __builtin_amdgcn_raw_buffer_load_b32(rKA, 0, wvoff, AUX_NT);
  pt[1] = __builtin_amdgcn_raw_buffer_load_b32(rVA, 0, wvoff, AUX_NT);
  pt[2] = __builtin_amdgcn_raw_buffer_load_b32(rWOA, 0, (512 * x8 + 16 * j) * 1024, AUX_NT);
  pt[3] = __builtin_amdgcn_raw_buffer_load_b32(rWOB, 0, (hB * 128 + 16 * wv) * 1024, AUX_NT);
  pt[4] = __builtin_amdgcn_raw_buffer_load_b32(rWS, 0, WS_YA + w * 2048, AUX_SC1);
  pt[5] = __builtin_amdgcn_raw_buffer_load_b32(rWS, 0, WS_YB + w * 2048, AUX_SC1);
  SB();
  v4u K[NSLOT][4], V[NSLOT][4];
  auto issue = [&](int u) __attribute__((always_inline)) {
    const int s = u >> 1;
    const bool isA = s == SA;
    const int tb_ = s < SA ? s : s - 1;
    if (u & 1) {
#pragma unroll
      for (int k = 0; k < 4; ++k) V[s][k] = ld128<V_AUX>(isA ? rVA : rVB, voff + k * 1024, wvoff + (isA ? 0 : ((tb_ + rot) & 15) * 32768));
    } else {
#pragma unroll
      for (int k = 0; k < 4; ++k) K[s][k] = ld128<K_AUX>(isA ? rKA : rKB, koff + k * 1024, wvoff + (isA ? 0 : ((tb_ + rot) & 15) * 32768));
    }
  };
  if (tid < 4) lds[L_FLAG + tid] = 0.f;
  __builtin_amdgcn_s_barrier();
  issue(0);
  SB();
  const unsigned tag = __builtin_amdgcn_readfirstlane(gen) + 1u;
  const unsigned tg4 = (tag & 0xffu) | (((tag + 85u) & 0xffu) << 8) | (((tag + 170u) & 0xffu) << 16) | (((tag + 37u) & 0xffu) << 24);
  v4u mask;
  {
    const unsigned hm = (n & 1) ? 0xffff0000u : 0x0000ffffu;
    const int q = (n & 7) >> 1;
    mask = v4u{q == 0 ? hm : 0u, q == 1 ? hm : 0u, q == 2 ? hm : 0u, q == 3 ? hm : 0u};
  }
  {
    f32x4 DB = {0.f, 0.f, 0.f, 0.f}, DA = {0.f, 0.f, 0.f, 0.f};
#pragma unroll
    for (int t = 0; t < 2; ++t) {
      DB = mfma(wqb[t], andm((unsigned)xbr[t] * 0x10001u, mask), DB);
      DA = mfma(wqa[t], andm((unsigned)xar[t] * 0x10001u, mask), DA);
    }
    if (n < 8) {
      *(f32x4*)&lds[L_QPART + wv * 128 + (g * 8 + n) * 4] = DB;
      *(f32x4*)&lds[L_QPART + 1024 + wv * 128 + (g * 8 + n) * 4] = DA;
    }
  }
  STAMP(24);
  __syncthreads();
  if (wv < 2) {
    const int u = lane & 31, c = 4 * u, hf = lane >> 5;
    const int e = ((c >> 5) * 8 + (c & 7)) * 4 + ((c >> 3) & 3);
    float y[4] = {0.f, 0.f, 0.f, 0.f};
#pragma unroll
    for (int k = 0; k < 4; ++k)
#pragma unroll
      for (int i = 0; i < 4; ++i) y[i] += lds[L_QPART + wv * 1024 + (4 * hf + k) * 128 + e + 4 * i];
#pragma unroll
    for (int i = 0; i < 4; ++i) y[i] = x32_sum(y[i]);
    const unsigned dst = wv == 0 ? WS_PQB + pB * 512 : WS_PQA + (hAq * 8 + ks) * 512;
    if (lane < 32) __builtin_amdgcn_raw_buffer_store_b128(pack4(y[0], y[1], y[2], y[3], tg4), rWS, dst + u * 16, 0, AUX_SC1);
  }
  STAMP(25);
  SB();
  issue(1);
  SB();
  PIN(K[0][3]);
  issue(2);
  SB();
  PIN(V[0][3]);
  STAMP(26);
  {
    const unsigned src = WS_PQB + (hB * 8 + wv) * 512 + (lane & 31) * 16;
    v4u pq = ld128<AUX_SC1>(rWS, src, 0);
    SB();
    issue(3);
    SB();
    PIN(K[1][3]);
    STAMP(28);
    issue(4);
    SB();
    AWAIT(__all(ok4(pq, tg4)), pq = ld128<AUX_SC1>(rWS, src, 0));
    if (lane < 32) *(f32x4*)&lds[L_QSUM + wv * 128 + 4 * lane] = un4(pq);
  }
  asm volatile("" :: "v"(pt[0]), "v"(pt[1]), "v"(pt[2]), "v"(pt[3]), "v"(pt[4]), "v"(pt[5]));
  STAMP(27);
  __syncthreads();
  v4u Bq[4];
  {
    f32x4 a = {0.f, 0.f, 0.f, 0.f}, b = {0.f, 0.f, 0.f, 0.f};
#pragma unroll
    for (int k = 0; k < 8; ++k) {
      a += *(const f32x4*)&lds[L_QSUM + k * 128 + 8 * (4 * g + (n & 3))];
      b += *(const f32x4*)&lds[L_QSUM + k * 128 + 8 * (4 * g + (n & 3)) + 4];
    }
    const v4u qB = v4u{cvtpk(a[0], a[1]), cvtpk(a[2], a[3]), cvtpk(b[0], b[1]), cvtpk(b[2], b[3])};
#pragma unroll
    for (int u = 0; u < 4; ++u) Bq[u] = ((n >> 2) == u) ? qB : v4u{0u, 0u, 0u, 0u};
  }
  STAMP(1);
  State st;
  st.m = -INFINITY; st.l = 0.f; st.acc = f32x4{0.f, 0.f, 0.f, 0.f};

  v4u ya_unit;
  auto outproj64 = [&](const float* of, const v4u (&wr)[2]) __attribute__((always_inline)) {
    f32x4 Y = {0.f, 0.f, 0.f, 0.f};
#pragma unroll
    for (int t = 0; t < 2; ++t) {
      const float o = of[8 * t + 2 * g + (n >> 3)];
      Y = mfma(wr[t], andm(split_rep(o, false), mask), Y);
      Y = mfma(wr[t], andm(split_rep(o, true), mask), Y);
    }
    const bool valid = (g >> 1) == (n >> 3);
    float z[4];
#pragma unroll
    for (int i = 0; i < 4; ++i) {
      float t = valid ? Y[i] : 0.f;
      t += dppf<ROR8>(t);
      z[i] = x32_sum(t);
    }
    const int i = 2 * (g >> 1) + (n >> 3);
    const float zi = (i & 2) ? ((i & 1) ? z[3] : z[2]) : ((i & 1) ? z[1] : z[0]);
    const float z1 = dppf<0x55>(zi), z2 = dppf<0xAA>(zi), z3 = dppf<0xFF>(zi);
    ya_unit = pack4(zi, z1, z2, z3, tg4);
  };
  const unsigned g2_base = WS_PA + ((x8 * 32 + (lane >> 2)) * 4 + (j >> 3)) * PA_REC;
  auto g2_load = [&]() __attribute__((always_inline)) {
#pragma unroll
    for (int k = 0; k < 2; ++k) {
      g2m[k] = ld128<AUX_SC1>(rWS, g2_base + k * 16 * 4 * PA_REC, 0);
      g2o[k] = ld128<AUX_SC1>(rWS, g2_base + k * 16 * 4 * PA_REC + 16 + (4 * (j & 7) + (lane & 3)) * 16, 0);
    }
  };
  auto g2_ok = [&]() __attribute__((always_inline)) {
    return (bool)__all(ok4(g2m[0], tg4) && ok4(g2m[1], tg4) && ok4(g2o[0], tg4) && ok4(g2o[1], tg4));
  };
  auto oa_merge = [&]() __attribute__((always_inline)) {
    const f32x4 ml0 = un4(g2m[0]), ml1 = un4(g2m[1]);
    float M = fmaxf(ml0[0], ml1[0]);
    M = fmaxf(M, dppf<ROR4>(M));
    M = fmaxf(M, dppf<ROR8>(M));
    M = xrow_max(M);
    const float f0 = __builtin_amdgcn_exp2f((ml0[0] - M) * C2), f1 = __builtin_amdgcn_exp2f((ml1[0] - M) * C2);
    float L = f0 * ml0[1] + f1 * ml1[1];
    f32x4 a = f0 * un4(g2o[0]) + f1 * un4(g2o[1]);
    L += dppf<ROR4>(L); L += dppf<ROR8>(L); L = xrow_sum(L);
#pragma unroll
    for (int i = 0; i < 4; ++i) { a[i] += dppf<ROR4>(a[i]); a[i] += dppf<ROR8>(a[i]); a[i] = xrow_sum(a[i]); }
    if (lane < 4) *(f32x4*)&lds[L_OFIN + 4 * lane] = a * (1.f / L);
    asm volatile("s_waitcnt lgkmcnt(0)" ::: "memory");
    if (lane == 0) __hip_atomic_store((unsigned*)&lds[L_FLAG + 1], 1u, __ATOMIC_RELAXED, __HIP_MEMORY_SCOPE_WORKGROUP);
  };
  auto ya_store = [&]() __attribute__((always_inline)) {
    if (!(n & 3))
      __builtin_amdgcn_raw_buffer_store_b128(ya_unit, rWS, WS_YA + w * 2048 + (64 * wv + 32 * (g & 1) + 16 * (g >> 1) + 8 * (n >> 3) + (n & 7)) * 4, 0, AUX_SC1);
  };
  bool oa_done = false, ya_done = false;

  auto slot = [&](auto S_) __attribute__((always_inline)) {
    constexpr int s = decltype(S_)::value;
    if constexpr (s != SA) {
      f32x4 S;
      qk_part(K[s], Bq, S);
      PIN(S);
      SB();
      if constexpr (next_unit(2 * s) >= 0) issue(next_unit(2 * s));
      SB();
      sm_pv(S, V[s], mask, n, st);
      PIN(st.acc); PIN(st.l);
      SB();
      if constexpr (next_unit(2 * s + 1) >= 0) issue(next_unit(2 * s + 1));
      SB();
    } else {
      issue(next_unit(2 * s)); issue(next_unit(2 * s + 1));
      SB();
      while (LDS_FLAG(2) != 2u) __builtin_amdgcn_s_sleep(1);
      MEMFENCE();
      State sa[4];
#pragma unroll
      for (int hd = 0; hd < 4; ++hd) {
        const v4u q = *(const v4u*)&lds[L_QA + hd * 64 + (4 * g + (n & 3)) * 4];
        v4u Ba[4];
#pragma unroll
        for (int u = 0; u < 4; ++u) Ba[u] = ((n >> 2) == u) ? q : v4u{0u, 0u, 0u, 0u};
        sa[hd].m = -INFINITY; sa[hd].l = 0.f; sa[hd].acc = f32x4{0.f, 0.f, 0.f, 0.f};
        f32x4 S;
        qk_part(K[s], Ba, S);
        sm_pv(S, V[s], mask, n, sa[hd]);
        finish_state(sa[hd]);
      }
#pragma unroll
      for (int hd = 0; hd < 4; ++hd) {
        float* rec = &lds[L_PARTA + (wv * 4 + hd) * 132];
        if (n < 8) {
#pragma unroll
          for (int i = 0; i < 4; ++i) rec[4 + 32 * g + 8 * i + n] = sa[hd].acc[i];
        }
        if (lane == 0) { rec[0] = sa[hd].m; rec[1] = sa[hd].l; }
      }
      asm volatile("s_waitcnt lgkmcnt(0)" ::: "memory");
      if (lane == 0) __hip_atomic_fetch_add((unsigned*)&lds[L_FLAG], 1u, __ATOMIC_RELAXED, __HIP_MEMORY_SCOPE_WORKGROUP);
      SB();
    }
    if constexpr (s == S_PA_C) {
      while (LDS_FLAG(0) != 8u) __builtin_amdgcn_s_sleep(1);
      MEMFENCE();
      const int hd = wv >> 1, u = 16 * (wv & 1) + n;
      float M = -INFINITY;
#pragma unroll
      for (int k = 0; k < 8; ++k) M = fmaxf(M, lds[L_PARTA + (k * 4 + hd) * 132]);
      const float* r0 = &lds[L_PARTA + (2 * g * 4 + hd) * 132];
      const float* r1 = r0 + 4 * 132;
      const float f0 = __builtin_amdgcn_exp2f((r0[0] - M) * C2), f1 = __builtin_amdgcn_exp2f((r1[0] - M) * C2);
      f32x4 o = f0 * *(const f32x4*)&r0[4 + 4 * u] + f1 * *(const f32x4*)&r1[4 + 4 * u];
      const float L = xrow_sum(f0 * r0[1] + f1 * r1[1]);
#pragma unroll
      for (int i = 0; i < 4; ++i) o[i] = xrow_sum(o[i]);
      const unsigned base = WS_PA + ((x8 * 32 + j) * 4 + hd) * PA_REC;
      if (lane < 16) __builtin_amdgcn_raw_buffer_store_b128(pack4(o[0], o[1], o[2], o[3], tg4), rWS, base + 16 + u * 16, 0, AUX_SC1);
      if (lane == 16 && !(wv & 1)) __builtin_amdgcn_raw_buffer_store_b128(pack4(M, L, 0.f, 0.f, tg4), rWS, base, 0, AUX_SC1);
      SB();
    }
    STAMP(2 + s);
    if constexpr (LOCKSTEP && s % (LOCKSTEP ? LOCKSTEP : 1) == LOCKSTEP - 1 && s + 1 < NSLOT) { SB(); __builtin_amdgcn_s_barrier(); SB(); }
    if constexpr (s == S_QA_G1) if (wv == 4) {
      g1 = ld128<AUX_SC1>(rWS, WS_PQA + (hAq * 8 + ((lane >> 2) & 7)) * 512 + (4 * ks + (lane & 3)) * 16, 0);
      SB();
    }
    if constexpr (s == S_QA_R) if (wv == 4) {
      AWAIT(__all(ok4(g1, tg4)), g1 = ld128<AUX_SC1>(rWS, WS_PQA + (hAq * 8 + ((lane >> 2) & 7)) * 512 + (4 * ks + (lane & 3)) * 16, 0));
      f32x4 v = un4(g1);
#pragma unroll
      for (int i = 0; i < 4; ++i) { v[i] += dppf<ROR4>(v[i]); v[i] += dppf<ROR8>(v[i]); v[i] = x16_sum(v[i]); }
      if (lane < 4)
        __builtin_amdgcn_raw_buffer_store_b128(v4u{cvtpk(v[0], v[1]), tag, cvtpk(v[2], v[3]), tag}, rWS, WS_QA + (x8 * 32 + j) * 128 + lane * 16, 0, AUX_SC1);
      SB();
    }
    if constexpr (s == S_QA_G2) if (wv < 2) {
      qa = ld128<AUX_SC1>(rWS, WS_QA + (x8 * 32 + 16 * wv + (lane >> 2)) * 128 + (lane & 3) * 16, 0);
      SB();
    }
    if constexpr (s == S_QA_L) if (wv < 2) {
      AWAIT(__all(tag2(qa, tag)), qa = ld128<AUX_SC1>(rWS, WS_QA + (x8 * 32 + 16 * wv + (lane >> 2)) * 128 + (lane & 3) * 16, 0));
      *(v2u*)&lds[L_QA + 2 * (64 * wv + lane)] = v2u{qa.x, qa.z};
      asm volatile("s_waitcnt lgkmcnt(0)" ::: "memory");
      if (lane == 0) __hip_atomic_fetch_add((unsigned*)&lds[L_FLAG + 2], 1u, __ATOMIC_RELAXED, __HIP_MEMORY_SCOPE_WORKGROUP);
      SB();
    }
    if constexpr (s == S_PA_G) if (wv == 7) { g2_load(); SB(); }
    if constexpr (s == S_PA_R) if (wv == 7) {
      if (g2_ok()) { oa_merge(); oa_done = true; }
      SB();
    }
    if constexpr (s == S_YA) {
      if (LDS_FLAG(1) == 1u) {
        MEMFENCE();
        outproj64(&lds[L_OFIN], woa);
        if (!YA_DEFER) ya_store();
        ya_done = true;
      }
      SB();
    }
    if constexpr (S_WO >= 0 && s >= S_WO && s < S_WO + 16 / WO_PER) {
#pragma unroll
      for (int k = WO_PER * (s - S_WO); k < WO_PER * (s - S_WO) + WO_PER; ++k)
        wob[k] = ld128<WO_AUX>(rWOB, g * 1024 + n * 16, (hB * 128 + 16 * wv + 4 * (k >> 2)) * 1024 + (k & 3) * 256);
      SB();
    }
    if constexpr (s == S_WOA) {
#pragma unroll
      for (int t = 0; t < 2; ++t)
        woa[t] = ld128<AUX_NT>(rWOA, (lane >> 3) * 1024 + (lane & 7) * 16, (512 * x8 + 16 * j + 8 * t) * 1024 + wv * 128);
      SB();
    }
  };
  run_slots(slot, std::make_integer_sequence<int, NSLOT>{});

  STAMP(19);
  if (YA_DEFER && ya_done) ya_store();
  finish_state(st);
  if (n < 8) {
#pragma unroll
    for (int i = 0; i < 4; ++i) lds[L_PARTB + wv * 128 + opos(32 * g + 8 * i + n)] = st.acc[i];
  }
  if (lane == 0) { lds[L_MLB + 2 * wv] = st.m; lds[L_MLB + 2 * wv + 1] = st.l; }
  __syncthreads();
  {
    float M = -INFINITY;
#pragma unroll
    for (int k = 0; k < 8; ++k) M = fmaxf(M, lds[L_MLB + 2 * k]);
    float L = 0.f;
    f32x4 o4 = {0.f, 0.f, 0.f, 0.f};
#pragma unroll
    for (int k = 0; k < 8; ++k) {
      const float f = __builtin_amdgcn_exp2f((lds[L_MLB + 2 * k] - M) * C2);
      L = fmaf(f, lds[L_MLB + 2 * k + 1], L);
      o4 += f * *(const f32x4*)&lds[L_PARTB + k * 128 + 16 * wv + 4 * g];
    }
    if (tid == 0) __builtin_amdgcn_raw_buffer_store_b128(pack4(M, L, 0.f, 0.f, tg4), rWS, WS_MLB + pB * 128, 0, AUX_SC1);
    f32x4 Y[4];
#pragma unroll
    for (int cb = 0; cb < 4; ++cb) Y[cb] = f32x4{0.f, 0.f, 0.f, 0.f};
#pragma unroll
    for (int rg = 0; rg < 4; ++rg) {
      const v4u fr = andm(split_rep(o4[rg], n >= 8), mask);
#pragma unroll
      for (int cb = 0; cb < 4; ++cb) Y[cb] = mfma(wob[rg * 4 + cb], fr, Y[cb]);
    }
#pragma unroll
    for (int cb = 0; cb < 4; ++cb) {
#pragma unroll
      for (int i = 0; i < 4; ++i) Y[cb][i] += dppf<ROR8>(Y[cb][i]);
      if (n < 8) *(f32x4*)&lds[L_YPART + wv * 512 + ((cb * 4 + g) * 8 + n) * 4] = Y[cb];
    }
  }
  __syncthreads();
  STAMP(20);
  if (tid < 256) {
    const int u = (wv << 5) | (lane & 31), c = 4 * u, hf = lane >> 5;
    const int idx = (((c >> 7) * 4 + ((c >> 5) & 3)) * 8 + (c & 7)) * 4 + ((c >> 3) & 3);
    float y[4] = {0.f, 0.f, 0.f, 0.f};
#pragma unroll
    for (int k = 0; k < 4; ++k)
#pragma unroll
      for (int i = 0; i < 4; ++i) y[i] += lds[L_YPART + (4 * hf + k) * 512 + idx + 4 * i];
#pragma unroll
    for (int i = 0; i < 4; ++i) y[i] = x32_sum(y[i]);
    if (lane < 32) __builtin_amdgcn_raw_buffer_store_b128(pack4(y[0], y[1], y[2], y[3], tg4), rWS, WS_YB + pB * 2048 + u * 16, 0, AUX_SC1);
  }
  {
    if (wv == 7 && !oa_done) {
      MEMFENCE();
      g2_load();
      AWAIT(g2_ok(), g2_load());
      oa_merge();
    }
    if (!ya_done) {
      while (LDS_FLAG(1) != 1u) __builtin_amdgcn_s_sleep(1);
      MEMFENCE();
      outproj64(&lds[L_OFIN], woa);
      ya_store();
    }
  }
  STAMP(21);
  if (wv < 4) {
    const bool isB = w >= 128;
    const unsigned yoff = (isB ? WS_YB : WS_YA) + tid * 2048 + (w & 127) * 16;
    v4u y = ld128<AUX_SC1>(rWS, yoff, 0), ml = pack4(0.f, 1.f, 0.f, 0.f, tg4);
    if (isB) ml = ld128<AUX_SC1>(rWS, WS_MLB + tid * 128, 0);
    for (unsigned spins = 0; spins < SPIN_CAP; ++spins) {
      const bool ok = ok4(y, tg4) && ok4(ml, tg4);
      if (__all(ok)) break;
      __builtin_amdgcn_s_sleep(POLL_SLEEP);
      MEMFENCE();
      if (!ok) {
        y = ld128<AUX_SC1>(rWS, yoff, 0);
        if (isB) ml = ld128<AUX_SC1>(rWS, WS_MLB + tid * 128, 0);
      }
    }
    STAMP(22);
    f32x4 v = un4(y);
    if (isB) {
      const f32x4 mlf = un4(ml);
      const float m = mlf[0];
      float M = fmaxf(m, dppf<XOR1>(m));
      M = fmaxf(M, dppf<XOR2>(M));
      M = fmaxf(M, dppf<HMIRROR>(M));
      const float f = __builtin_amdgcn_exp2f((m - M) * C2);
      float L = f * mlf[1];
      L += dppf<XOR1>(L); L += dppf<XOR2>(L); L += dppf<HMIRROR>(L);
      v *= f / L;
    }
#pragma unroll
    for (int i = 0; i < 4; ++i) {
      float t = v[i];
      t += dppf<XOR1>(t); t += dppf<XOR2>(t); t += dppf<ROR4>(t); t += dppf<ROR8>(t);
      v[i] = xrow_sum(t);
    }
    if (lane == 0) *(f32x4*)&lds[L_RED + 4 * wv] = v;
  }
  __syncthreads();
  if (tid == 0) {
    const f32x4* r = (const f32x4*)&lds[L_RED];
    const f32x4 s = (r[0] + r[1]) + (r[2] + r[3]);
    *(v2u*)(out + (w >= 128 ? 512 : 0) + 4 * (w & 127)) = v2u{cvtpk(s[0], s[1]), cvtpk(s[2], s[3])};
    if (w == 128) __builtin_amdgcn_raw_buffer_store_b32(tag, rWS, WS_GEN, 0, AUX_SC1);
  }
}

}

extern "C" void kernel_launch(void* const* d_in, const int* in_sizes, int n_in,
                              void* d_out, int out_size, void* d_ws, size_t ws_size,
                              hipStream_t stream) {
  (void)in_sizes; (void)n_in; (void)out_size; (void)ws_size;
  fused_decode<<<256, 512, 0, stream>>>(
      (const uint16_t*)d_in[0], (const uint16_t*)d_in[1], (const uint16_t*)d_in[2], (const uint16_t*)d_in[3], (const uint16_t*)d_in[4],
      (const uint16_t*)d_in[5], (const uint16_t*)d_in[6], (const uint16_t*)d_in[7], (const uint16_t*)d_in[8], (const uint16_t*)d_in[9],
      (uint16_t*)d_out, (unsigned char*)d_ws);
}
```

```cpp
#include <hip/hip_runtime.h>
#include <hip/hip_bf16.h>
#include <stdint.h>
#include <utility>

#define GAS __attribute__((address_space(1)))
typedef unsigned v4u __attribute__((ext_vector_type(4)));
typedef unsigned v2u __attribute__((ext_vector_type(2)));
typedef float f32x4 __attribute__((ext_vector_type(4)));
typedef float f32x2 __attribute__((ext_vector_type(2)));
typedef short bf16x8 __attribute__((ext_vector_type(8)));
typedef __bf16 bf16x2_t __attribute__((ext_vector_type(2)));
typedef __amdgpu_buffer_rsrc_t rsrc_t;

namespace {

constexpr int DIST = 4;
constexpr int LOCKSTEP = 2;
constexpr int K_AUX = 2, V_AUX = 2, WQ_AUX = 2, WO_AUX = 2;
constexpr int AUX_NT = 2, AUX_SC1 = 16;
constexpr int RSRC_W3 = 0x00020000;
constexpr float SCALE = 0.08838834764831845f;
constexpr float C2 = 1.4426950408889634f;
constexpr int SK = 16384, WIN0 = 16384 - 4096;
constexpr int NSLOT = 17;
constexpr int PRE = 5;
constexpr int next_unit(int u) { return (u + DIST >= PRE && u + DIST < 2 * NSLOT) ? u + DIST : -1; }
constexpr int NBUF = (DIST + 1) / 2;
constexpr int S_QA_G1 = 0, S_QA_R = S_QA_G1 + NBUF;
constexpr int S_QA_G2 = 5, S_QA_L = S_QA_G2 + NBUF;
constexpr int SA = S_QA_L + 1;
constexpr int S_PA_C = SA + 2;
constexpr int S_PA_G = 12, S_PA_R = S_PA_G + NBUF;
constexpr int S_WO = 1, WO_PER = 2, S_WOA = 13;
constexpr int S_YA = S_PA_R + 1;
static_assert(S_YA < NSLOT && S_PA_C < S_PA_G, "schedule");
constexpr unsigned SPIN_CAP = 1u << 18;

constexpr unsigned WS_GEN = 0;
constexpr unsigned WS_PQB = 4096;
constexpr unsigned WS_PQA = WS_PQB + 256 * 512;
constexpr unsigned WS_QA = WS_PQA + 256 * 512;
constexpr unsigned PA_REC = 640;
constexpr unsigned WS_PA = WS_QA + 8 * 32 * 128;
constexpr unsigned WS_MLB = WS_PA + 8 * 32 * 4 * PA_REC;
constexpr unsigned WS_YA = WS_MLB + 256 * 128;
constexpr unsigned WS_YB = WS_YA + 256 * 2048;
constexpr unsigned WS_END = WS_YB + 256 * 2048;

constexpr int L_QPART = 0;
constexpr int L_QSUM = L_QPART + 2 * 8 * 128;
constexpr int L_PARTA = L_QSUM + 8 * 128;
constexpr int L_PARTB = L_PARTA + 8 * 4 * 132;
constexpr int L_MLB = L_PARTB + 8 * 128;
constexpr int L_YPART = L_MLB + 16;
constexpr int L_RED = L_YPART + 8 * 512;
constexpr int L_OFIN = L_RED + 16;
constexpr int L_QA = L_OFIN + 16;
constexpr int L_FLAG = L_QA + 256;
constexpr int L_END = L_FLAG + 4;

__device__ __forceinline__ rsrc_t mk_rsrc(const void* p, unsigned bytes) {
  return __builtin_amdgcn_make_buffer_rsrc(const_cast<void*>(p), 0, bytes, RSRC_W3);
}
template <int AUX> __device__ __forceinline__ v4u ld128(rsrc_t rs, unsigned voff, unsigned soff) {
  return __builtin_amdgcn_raw_buffer_load_b128(rs, voff, soff, AUX);
}
__device__ __forceinline__ f32x4 mfma(v4u a, v4u b, f32x4 c) {
  return __builtin_amdgcn_mfma_f32_16x16x32_bf16(__builtin_bit_cast(bf16x8, a), __builtin_bit_cast(bf16x8, b), c, 0, 0, 0);
}
__device__ __forceinline__ unsigned cvtpk(float lo, float hi) {
  f32x2 v = {lo, hi};
  return __builtin_bit_cast(unsigned, __builtin_convertvector(v, bf16x2_t));
}
template <int CTRL> __device__ __forceinline__ float dppf(float x) {
  return __builtin_bit_cast(float, __builtin_amdgcn_mov_dpp(__builtin_bit_cast(int, x), CTRL, 0xf, 0xf, true));
}
template <int CTRL, int RM, int BM> __device__ __forceinline__ unsigned dppu(unsigned old, unsigned x) {
  return (unsigned)__builtin_amdgcn_update_dpp((int)old, (int)x, CTRL, RM, BM, false);
}
constexpr int XOR1 = 0xB1, XOR2 = 0x4E, HMIRROR = 0x141, ROR8 = 0x128, ROR4 = 0x124, BCAST = 0x150;
__device__ __forceinline__ float x16_sum(float x) {
  auto s = __builtin_amdgcn_permlane16_swap(__float_as_uint(x), __float_as_uint(x), false, false);
  return __uint_as_float(s[0]) + __uint_as_float(s[1]);
}
__device__ __forceinline__ float x32_sum(float x) {
  auto t = __builtin_amdgcn_permlane32_swap(__float_as_uint(x), __float_as_uint(x), false, false);
  return __uint_as_float(t[0]) + __uint_as_float(t[1]);
}
__device__ __forceinline__ float xrow_max(float x) {
  auto s = __builtin_amdgcn_permlane16_swap(__float_as_uint(x), __float_as_uint(x), false, false);
  x = fmaxf(__uint_as_float(s[0]), __uint_as_float(s[1]));
  auto t = __builtin_amdgcn_permlane32_swap(__float_as_uint(x), __float_as_uint(x), false, false);
  return fmaxf(__uint_as_float(t[0]), __uint_as_float(t[1]));
}
__device__ __forceinline__ float xrow_sum(float x) { return x32_sum(x16_sum(x)); }
__device__ __forceinline__ v4u andm(unsigned rep, const v4u& m) { return v4u{rep & m.x, rep & m.y, rep & m.z, rep & m.w}; }
__device__ __forceinline__ unsigned split_rep(float x, bool take_lo) {
  const unsigned hi = cvtpk(x, x);
  const float r = x - __uint_as_float(hi & 0xffff0000u);
  const unsigned lo = cvtpk(r, r);
  return take_lo ? lo : hi;
}
__device__ __forceinline__ unsigned tb(float v, unsigned byte) { return ((__float_as_uint(v) + 0x80u) & 0xffffff00u) | byte; }
__device__ __forceinline__ v4u pack4(float a, float b, float c, float d, unsigned tag) {
  return v4u{tb(a, tag & 0xffu), tb(b, (tag >> 8) & 0xffu), tb(c, (tag >> 16) & 0xffu), tb(d, tag >> 24)};
}
__device__ __forceinline__ bool ok4(const v4u& x, unsigned tag) {
  return (((x.x ^ tag) | (x.y ^ (tag >> 8)) | (x.z ^ (tag >> 16)) | (x.w ^ (tag >> 24))) & 0xffu) == 0u;
}
__device__ __forceinline__ f32x4 un4(const v4u& x) {
  return f32x4{__uint_as_float(x.x & 0xffffff00u), __uint_as_float(x.y & 0xffffff00u), __uint_as_float(x.z & 0xffffff00u),
               __uint_as_float(x.w & 0xffffff00u)};
}
__device__ __forceinline__ bool tag2(const v4u& x, unsigned tag) { return x.y == tag && x.w == tag; }

#define SB() __builtin_amdgcn_sched_barrier(0)
#define MEMFENCE() asm volatile("" ::: "memory")
#define PIN(x) asm volatile("" : "+v"(x) :: "memory")
#define AWAIT(OK, RELOAD) do { if (__builtin_expect(!(OK), 0)) { unsigned spins_ = 0; \
    do { __builtin_amdgcn_s_sleep(2); MEMFENCE(); RELOAD; } while (!(OK) && ++spins_ < SPIN_CAP); } } while (0)
#define LDS_FLAG(i) __hip_atomic_load((unsigned*)&lds[L_FLAG + (i)], __ATOMIC_RELAXED, __HIP_MEMORY_SCOPE_WORKGROUP)

template <typename F, int... I>
__device__ __forceinline__ void run_slots(F& f, std::integer_sequence<int, I...>) { (f(std::integral_constant<int, I>{}), ...); }

struct State { float m, l; f32x4 acc; };

__device__ __forceinline__ void qk_part(const v4u (&K)[4], const v4u (&Bq)[4], f32x4& S) {
  S = f32x4{0.f, 0.f, 0.f, 0.f};
#pragma unroll
  for (int u = 0; u < 4; ++u) S = mfma(K[u], Bq[u], S);
}
__device__ __forceinline__ void sm_pv(const f32x4& S, const v4u (&V)[4], const v4u& mask, int n, State& st) {
  const float s01 = (n & 1) ? S[1] : S[0], s23 = (n & 1) ? S[3] : S[2];
  float s = (n & 2) ? s23 : s01;
  s += dppf<XOR1>(s);
  s += dppf<XOR2>(s);
  s = __uint_as_float(cvtpk(s, s) & 0xffff0000u) * SCALE;
  s = __uint_as_float(cvtpk(s, s) & 0xffff0000u);
  float tm = fmaxf(s, dppf<ROR4>(s));
  tm = fmaxf(tm, dppf<ROR8>(tm));
  tm = xrow_max(tm);
  const float mn = fmaxf(st.m, tm);
  const float alpha = __builtin_amdgcn_exp2f((st.m - mn) * C2);
  const float p = __builtin_amdgcn_exp2f((s - mn) * C2);
  st.m = mn;
  st.l = fmaf(st.l, alpha, p);
  st.acc *= alpha;
  const unsigned pk = split_rep(p, (n & 2) != 0);
  unsigned rep;
#define PV_STEP(u) rep = dppu<BCAST + 4 * (u), 0xf, 0x3>(pk, pk); rep = dppu<BCAST + 4 * (u) + 2, 0xf, 0xc>(rep, pk); \
  st.acc = mfma(V[u], andm(rep, mask), st.acc);
  PV_STEP(0) PV_STEP(1) PV_STEP(2) PV_STEP(3)
#undef PV_STEP
}
__device__ __forceinline__ void finish_state(State& st) {
  float l = st.l;
  l += dppf<ROR4>(l);
  l += dppf<ROR8>(l);
  st.l = xrow_sum(l);
#pragma unroll
  for (int i = 0; i < 4; ++i) st.acc[i] += dppf<ROR8>(st.acc[i]);
}
__device__ __forceinline__ int opos(int d) { return (d >> 4) * 16 + (d & 3) * 4 + ((d >> 2) & 3); }

__global__ void __launch_bounds__(512, 2)
fused_decode(const uint16_t* __restrict__ xA, const uint16_t* __restrict__ kA, const uint16_t* __restrict__ vA,
             const uint16_t* __restrict__ wqA, const uint16_t* __restrict__ woA,
             const uint16_t* __restrict__ xB, const uint16_t* __restrict__ kB, const uint16_t* __restrict__ vB,
             const uint16_t* __restrict__ wqB, const uint16_t* __restrict__ woB,
             uint16_t* __restrict__ out, unsigned char* ws) {
  __shared__ __attribute__((aligned(16))) float lds[L_END];
  const int tid = threadIdx.x, lane = tid & 63, wv = __builtin_amdgcn_readfirstlane(tid >> 6);
  const int g = lane >> 4, n = lane & 15;
  const int w = blockIdx.x, x8 = w & 7, j = w >> 3;
  const int hB = 4 * x8 + (j & 3), sB = j >> 2;
  const int pB = hB * 8 + sB;
  const int hAq = 4 * x8 + (j >> 3), ks = j & 7;

  const rsrc_t rKB = mk_rsrc(kB + ((size_t)hB * SK + sB * 2048) * 128, 2048 * 256);
  const rsrc_t rVB = mk_rsrc(vB + ((size_t)hB * SK + sB * 2048) * 128, 2048 * 256);
  const rsrc_t rKA = mk_rsrc(kA + ((size_t)x8 * SK + WIN0 + j * 128) * 128, 128 * 256);
  const rsrc_t rVA = mk_rsrc(vA + ((size_t)x8 * SK + WIN0 + j * 128) * 128, 128 * 256);
  const rsrc_t rWQB = mk_rsrc(wqB, 512 * 8192);
  const rsrc_t rWQA = mk_rsrc(wqA, 512 * 8192);
  const rsrc_t rWOB = mk_rsrc(woB, 4096 * 1024);
  const rsrc_t rWOA = mk_rsrc(woA, 4096 * 1024);
  const rsrc_t rXB = mk_rsrc(xB, 1024);
  const rsrc_t rXA = mk_rsrc(xA, 1024);
  const rsrc_t rWS = mk_rsrc(ws, WS_END);

  const unsigned koff = (n >> 2) * 256 + g * 64 + (n & 3) * 16, voff = lane * 16;
  const unsigned wvoff = wv * 4096;
  const int rot = (w >> 3) & 15;

  v4u qa, g1, g2o[2], g2m[2], woa[2], wob[16];
  v4u wqb[2], wqa[2];
  unsigned short xbr[2], xar[2];
#pragma unroll
  for (int t = 0; t < 2; ++t) {
    wqb[t] = ld128<WQ_AUX>(rWQB, g * 8192 + n * 16, (64 * sB + 8 * wv + 4 * t) * 8192 + hB * 256);
    xbr[t] = __builtin_amdgcn_raw_buffer_load_b16(rXB, g * 2, (64 * sB + 8 * wv + 4 * t) * 2, 0);
  }
#pragma unroll
  for (int t = 0; t < 2; ++t) {
    wqa[t] = ld128<WQ_AUX>(rWQA, g * 8192 + n * 16, (64 * ks + 8 * wv + 4 * t) * 8192 + hAq * 256);
    xar[t] = __builtin_amdgcn_raw_buffer_load_b16(rXA, g * 2, (64 * ks + 8 * wv + 4 * t) * 2, 0);
  }
  const unsigned gen = __builtin_amdgcn_raw_buffer_load_b32(rWS, WS_GEN, 0, AUX_SC1);
  unsigned pt[6];
  pt[0] = __builtin_amdgcn_raw_buffer_load_b32(rKA, 0, wvoff, AUX_NT);
  pt[1] = __builtin_amdgcn_raw_buffer_load_b32(rVA, 0, wvoff, AUX_NT);
  pt[2] = __builtin_amdgcn_raw_buffer_load_b32(rWOA, 0, (512 * x8 + 16 * j) * 1024, AUX_NT);
  pt[3] = __builtin_amdgcn_raw_buffer_load_b32(rWOB, 0, (hB * 128 + 16 * wv) * 1024, AUX_NT);
  pt[4] = __builtin_amdgcn_raw_buffer_load_b32(rWS, 0, WS_YA + w * 2048, AUX_SC1);
  pt[5] = __builtin_amdgcn_raw_buffer_load_b32(rWS, 0, WS_YB + w * 2048, AUX_SC1);
  SB();
  v4u K[NSLOT][4], V[NSLOT][4];
  auto issue = [&](int u) __attribute__((always_inline)) {
    const int s = u >> 1;
    const bool isA = s == SA;
    const int tb_ = s < SA ? s : s - 1;
    if (u & 1) {
#pragma unroll
      for (int k = 0; k < 4; ++k) V[s][k] = ld128<V_AUX>(isA ? rVA : rVB, voff + k * 1024, wvoff + (isA ? 0 : ((tb_ + rot) & 15) * 32768));
    } else {
#pragma unroll
      for (int k = 0; k < 4; ++k) K[s][k] = ld128<K_AUX>(isA ? rKA : rKB, koff + k * 1024, wvoff + (isA ? 0 : ((tb_ + rot) & 15) * 32768));
    }
  };
  if (tid < 4) lds[L_FLAG + tid] = 0.f;
  __builtin_amdgcn_s_barrier();
  issue(0);
  SB();
  const unsigned tag = __builtin_amdgcn_readfirstlane(gen) + 1u;
  const unsigned tg4 = (tag & 0xffu) | (((tag + 85u) & 0xffu) << 8) | (((tag + 170u) & 0xffu) << 16) | (((tag + 37u) & 0xffu) << 24);
  v4u mask;
  {
    const unsigned hm = (n & 1) ? 0xffff0000u : 0x0000ffffu;
    const int q = (n & 7) >> 1;
    mask = v4u{q == 0 ? hm : 0u, q == 1 ? hm : 0u, q == 2 ? hm : 0u, q == 3 ? hm : 0u};
  }
  {
    f32x4 DB = {0.f, 0.f, 0.f, 0.f}, DA = {0.f, 0.f, 0.f, 0.f};
#pragma unroll
    for (int t = 0; t < 2; ++t) {
      DB = mfma(wqb[t], andm((unsigned)xbr[t] * 0x10001u, mask), DB);
      DA = mfma(wqa[t], andm((unsigned)xar[t] * 0x10001u, mask), DA);
    }
    if (n < 8) {
      *(f32x4*)&lds[L_QPART + wv * 128 + (g * 8 + n) * 4] = DB;
      *(f32x4*)&lds[L_QPART + 1024 + wv * 128 + (g * 8 + n) * 4] = DA;
    }
  }
  __syncthreads();
  if (wv < 2) {
    const int u = lane & 31, c = 4 * u, hf = lane >> 5;
    const int e = ((c >> 5) * 8 + (c & 7)) * 4 + ((c >> 3) & 3);
    float y[4] = {0.f, 0.f, 0.f, 0.f};
#pragma unroll
    for (int k = 0; k < 4; ++k)
#pragma unroll
      for (int i = 0; i < 4; ++i) y[i] += lds[L_QPART + wv * 1024 + (4 * hf + k) * 128 + e + 4 * i];
#pragma unroll
    for (int i = 0; i < 4; ++i) y[i] = x32_sum(y[i]);
    const unsigned dst = wv == 0 ? WS_PQB + pB * 512 : WS_PQA + (hAq * 8 + ks) * 512;
    if (lane < 32) __builtin_amdgcn_raw_buffer_store_b128(pack4(y[0], y[1], y[2], y[3], tg4), rWS, dst + u * 16, 0, AUX_SC1);
  }
  SB();
  issue(1);
  SB();
  PIN(K[0][3]);
  issue(2);
  SB();
  PIN(V[0][3]);
  {
    const unsigned src = WS_PQB + (hB * 8 + wv) * 512 + (lane & 31) * 16;
    v4u pq = ld128<AUX_SC1>(rWS, src, 0);
    SB();
    issue(3);
    SB();
    PIN(K[1][3]);
    issue(4);
    SB();
    AWAIT(__all(ok4(pq, tg4)), pq = ld128<AUX_SC1>(rWS, src, 0));
    if (lane < 32) *(f32x4*)&lds[L_QSUM + wv * 128 + 4 * lane] = un4(pq);
  }
  asm volatile("" :: "v"(pt[0]), "v"(pt[1]), "v"(pt[2]), "v"(pt[3]), "v"(pt[4]), "v"(pt[5]));
  __syncthreads();
  v4u Bq[4];
  {
    f32x4 a = {0.f, 0.f, 0.f, 0.f}, b = {0.f, 0.f, 0.f, 0.f};
#pragma unroll
    for (int k = 0; k < 8; ++k) {
      a += *(const f32x4*)&lds[L_QSUM + k * 128 + 8 * (4 * g + (n & 3))];
      b += *(const f32x4*)&lds[L_QSUM + k * 128 + 8 * (4 * g + (n & 3)) + 4];
    }
    const v4u qB = v4u{cvtpk(a[0], a[1]), cvtpk(a[2], a[3]), cvtpk(b[0], b[1]), cvtpk(b[2], b[3])};
#pragma unroll
    for (int u = 0; u < 4; ++u) Bq[u] = ((n >> 2) == u) ? qB : v4u{0u, 0u, 0u, 0u};
  }
  State st;
  st.m = -INFINITY; st.l = 0.f; st.acc = f32x4{0.f, 0.f, 0.f, 0.f};

  v4u ya_unit;
  auto outproj64 = [&](const float* of, const v4u (&wr)[2]) __attribute__((always_inline)) {
    f32x4 Y = {0.f, 0.f, 0.f, 0.f};
#pragma unroll
    for (int t = 0; t < 2; ++t) {
      const float o = of[8 * t + 2 * g + (n >> 3)];
      Y = mfma(wr[t], andm(split_rep(o, false), mask), Y);
      Y = mfma(wr[t], andm(split_rep(o, true), mask), Y);
    }
    const bool valid = (g >> 1) == (n >> 3);
    float z[4];
#pragma unroll
    for (int i = 0; i < 4; ++i) {
      float t = valid ? Y[i] : 0.f;
      t += dppf<ROR8>(t);
      z[i] = x32_sum(t);
    }
    const int i = 2 * (g >> 1) + (n >> 3);
    const float zi = (i & 2) ? ((i & 1) ? z[3] : z[2]) : ((i & 1) ? z[1] : z[0]);
    const float z1 = dppf<0x55>(zi), z2 = dppf<0xAA>(zi), z3 = dppf<0xFF>(zi);
    ya_unit = pack4(zi, z1, z2, z3, tg4);
  };
  const unsigned g2_base = WS_PA + ((x8 * 32 + (lane >> 2)) * 4 + (j >> 3)) * PA_REC;
  auto g2_load = [&]() __attribute__((always_inline)) {
#pragma unroll
    for (int k = 0; k < 2; ++k) {
      g2m[k] = ld128<AUX_SC1>(rWS, g2_base + k * 16 * 4 * PA_REC, 0);
      g2o[k] = ld128<AUX_SC1>(rWS, g2_base + k * 16 * 4 * PA_REC + 16 + (4 * (j & 7) + (lane & 3)) * 16, 0);
    }
  };
  auto g2_ok = [&]() __attribute__((always_inline)) {
    return (bool)__all(ok4(g2m[0], tg4) && ok4(g2m[1], tg4) && ok4(g2o[0], tg4) && ok4(g2o[1], tg4));
  };
  auto oa_merge = [&]() __attribute__((always_inline)) {
    const f32x4 ml0 = un4(g2m[0]), ml1 = un4(g2m[1]);
    float M = fmaxf(ml0[0], ml1[0]);
    M = fmaxf(M, dppf<ROR4>(M));
    M = fmaxf(M, dppf<ROR8>(M));
    M = xrow_max(M);
    const float f0 = __builtin_amdgcn_exp2f((ml0[0] - M) * C2), f1 = __builtin_amdgcn_exp2f((ml1[0] - M) * C2);
    float L = f0 * ml0[1] + f1 * ml1[1];
    f32x4 a = f0 * un4(g2o[0]) + f1 * un4(g2o[1]);
    L += dppf<ROR4>(L); L += dppf<ROR8>(L); L = xrow_sum(L);
#pragma unroll
    for (int i = 0; i < 4; ++i) { a[i] += dppf<ROR4>(a[i]); a[i] += dppf<ROR8>(a[i]); a[i] = xrow_sum(a[i]); }
    if (lane < 4) *(f32x4*)&lds[L_OFIN + 4 * lane] = a * (1.f / L);
    asm volatile("s_waitcnt lgkmcnt(0)" ::: "memory");
    if (lane == 0) __hip_atomic_store((unsigned*)&lds[L_FLAG + 1], 1u, __ATOMIC_RELAXED, __HIP_MEMORY_SCOPE_WORKGROUP);
  };
  auto ya_store = [&]() __attribute__((always_inline)) {
    if (!(n & 3))
      __builtin_amdgcn_raw_buffer_store_b128(ya_unit, rWS, WS_YA + w * 2048 + (64 * wv + 32 * (g & 1) + 16 * (g >> 1) + 8 * (n >> 3) + (n & 7)) * 4, 0, AUX_SC1);
  };
  bool oa_done = false, ya_done = false;

  auto slot = [&](auto S_) __attribute__((always_inline)) {
    constexpr int s = decltype(S_)::value;
    if constexpr (s != SA) {
      f32x4 S;
      qk_part(K[s], Bq, S);
      PIN(S);
      SB();
      if constexpr (next_unit(2 * s) >= 0) issue(next_unit(2 * s));
      SB();
      sm_pv(S, V[s], mask, n, st);
      PIN(st.acc); PIN(st.l);
      SB();
      if constexpr (next_unit(2 * s + 1) >= 0) issue(next_unit(2 * s + 1));
      SB();
    } else {
      issue(next_unit(2 * s)); issue(next_unit(2 * s + 1));
      SB();
      while (LDS_FLAG(2) != 2u) __builtin_amdgcn_s_sleep(1);
      MEMFENCE();
      State sa[4];
#pragma unroll
      for (int hd = 0; hd < 4; ++hd) {
        const v4u q = *(const v4u*)&lds[L_QA + hd * 64 + (4 * g + (n & 3)) * 4];
        v4u Ba[4];
#pragma unroll
        for (int u = 0; u < 4; ++u) Ba[u] = ((n >> 2) == u) ? q : v4u{0u, 0u, 0u, 0u};
        sa[hd].m = -INFINITY; sa[hd].l = 0.f; sa[hd].acc = f32x4{0.f, 0.f, 0.f, 0.f};
        f32x4 S;
        qk_part(K[s], Ba, S);
        sm_pv(S, V[s], mask, n, sa[hd]);
        finish_state(sa[hd]);
      }
#pragma unroll
      for (int hd = 0; hd < 4; ++hd) {
        float* rec = &lds[L_PARTA + (wv * 4 + hd) * 132];
        if (n < 8) {
#pragma unroll
          for (int i = 0; i < 4; ++i) rec[4 + 32 * g + 8 * i + n] = sa[hd].acc[i];
        }
        if (lane == 0) { rec[0] = sa[hd].m; rec[1] = sa[hd].l; }
      }
      asm volatile("s_waitcnt lgkmcnt(0)" ::: "memory");
      if (lane == 0) __hip_atomic_fetch_add((unsigned*)&lds[L_FLAG], 1u, __ATOMIC_RELAXED, __HIP_MEMORY_SCOPE_WORKGROUP);
      SB();
    }
    if constexpr (s == S_PA_C) {
      while (LDS_FLAG(0) != 8u) __builtin_amdgcn_s_sleep(1);
      MEMFENCE();
      const int hd = wv >> 1, u = 16 * (wv & 1) + n;
      float M = -INFINITY;
#pragma unroll
      for (int k = 0; k < 8; ++k) M = fmaxf(M, lds[L_PARTA + (k * 4 + hd) * 132]);
      const float* r0 = &lds[L_PARTA + (2 * g * 4 + hd) * 132];
      const float* r1 = r0 + 4 * 132;
      const float f0 = __builtin_amdgcn_exp2f((r0[0] - M) * C2), f1 = __builtin_amdgcn_exp2f((r1[0] - M) * C2);
      f32x4 o = f0 * *(const f32x4*)&r0[4 + 4 * u] + f1 * *(const f32x4*)&r1[4 + 4 * u];
      const float L = xrow_sum(f0 * r0[1] + f1 * r1[1]);
#pragma unroll
      for (int i = 0; i < 4; ++i) o[i] = xrow_sum(o[i]);
      const unsigned base = WS_PA + ((x8 * 32 + j) * 4 + hd) * PA_REC;
      if (lane < 16) __builtin_amdgcn_raw_buffer_store_b128(pack4(o[0], o[1], o[2], o[3], tg4), rWS, base + 16 + u * 16, 0, AUX_SC1);
      if (lane == 16 && !(wv & 1)) __builtin_amdgcn_raw_buffer_store_b128(pack4(M, L, 0.f, 0.f, tg4), rWS, base, 0, AUX_SC1);
      SB();
    }
    if constexpr (s % LOCKSTEP == LOCKSTEP - 1 && s + 1 < NSLOT) { SB(); __builtin_amdgcn_s_barrier(); SB(); }
    if constexpr (s == S_QA_G1) if (wv == 4) {
      g1 = ld128<AUX_SC1>(rWS, WS_PQA + (hAq * 8 + ((lane >> 2) & 7)) * 512 + (4 * ks + (lane & 3)) * 16, 0);
      SB();
    }
    if constexpr (s == S_QA_R) if (wv == 4) {
      AWAIT(__all(ok4(g1, tg4)), g1 = ld128<AUX_SC1>(rWS, WS_PQA + (hAq * 8 + ((lane >> 2) & 7)) * 512 + (4 * ks + (lane & 3)) * 16, 0));
      f32x4 v = un4(g1);
#pragma unroll
      for (int i = 0; i < 4; ++i) { v[i] += dppf<ROR4>(v[i]); v[i] += dppf<ROR8>(v[i]); v[i] = x16_sum(v[i]); }
      if (lane < 4)
        __builtin_amdgcn_raw_buffer_store_b128(v4u{cvtpk(v[0], v[1]), tag, cvtpk(v[2], v[3]), tag}, rWS, WS_QA + (x8 * 32 + j) * 128 + lane * 16, 0, AUX_SC1);
      SB();
    }
    if constexpr (s == S_QA_G2) if (wv < 2) {
      qa = ld128<AUX_SC1>(rWS, WS_QA + (x8 * 32 + 16 * wv + (lane >> 2)) * 128 + (lane & 3) * 16, 0);
      SB();
    }
    if constexpr (s == S_QA_L) if (wv < 2) {
      AWAIT(__all(tag2(qa, tag)), qa = ld128<AUX_SC1>(rWS, WS_QA + (x8 * 32 + 16 * wv + (lane >> 2)) * 128 + (lane & 3) * 16, 0));
      *(v2u*)&lds[L_QA + 2 * (64 * wv + lane)] = v2u{qa.x, qa.z};
      asm volatile("s_waitcnt lgkmcnt(0)" ::: "memory");
      if (lane == 0) __hip_atomic_fetch_add((unsigned*)&lds[L_FLAG + 2], 1u, __ATOMIC_RELAXED, __HIP_MEMORY_SCOPE_WORKGROUP);
      SB();
    }
    if constexpr (s == S_PA_G) if (wv == 7) { g2_load(); SB(); }
    if constexpr (s == S_PA_R) if (wv == 7) {
      if (g2_ok()) { oa_merge(); oa_done = true; }
      SB();
    }
    if constexpr (s == S_YA) {
      if (LDS_FLAG(1) == 1u) {
        MEMFENCE();
        outproj64(&lds[L_OFIN], woa);
        ya_done = true;
      }
      SB();
    }
    if constexpr (s >= S_WO && s < S_WO + 16 / WO_PER) {
#pragma unroll
      for (int k = WO_PER * (s - S_WO); k < WO_PER * (s - S_WO) + WO_PER; ++k)
        wob[k] = ld128<WO_AUX>(rWOB, g * 1024 + n * 16, (hB * 128 + 16 * wv + 4 * (k >> 2)) * 1024 + (k & 3) * 256);
      SB();
    }
    if constexpr (s == S_WOA) {
#pragma unroll
      for (int t = 0; t < 2; ++t)
        woa[t] = ld128<AUX_NT>(rWOA, (lane >> 3) * 1024 + (lane & 7) * 16, (512 * x8 + 16 * j + 8 * t) * 1024 + wv * 128);
      SB();
    }
  };
  run_slots(slot, std::make_integer_sequence<int, NSLOT>{});

  if (ya_done) ya_store();
  finish_state(st);
  if (n < 8) {
#pragma unroll
    for (int i = 0; i < 4; ++i) lds[L_PARTB + wv * 128 + opos(32 * g + 8 * i + n)] = st.acc[i];
  }
  if (lane == 0) { lds[L_MLB + 2 * wv] = st.m; lds[L_MLB + 2 * wv + 1] = st.l; }
  __syncthreads();
  {
    float M = -INFINITY;
#pragma unroll
    for (int k = 0; k < 8; ++k) M = fmaxf(M, lds[L_MLB + 2 * k]);
    float L = 0.f;
    f32x4 o4 = {0.f, 0.f, 0.f, 0.f};
#pragma unroll
    for (int k = 0; k < 8; ++k) {
      const float f = __builtin_amdgcn_exp2f((lds[L_MLB + 2 * k] - M) * C2);
      L = fmaf(f, lds[L_MLB + 2 * k + 1], L);
      o4 += f * *(const f32x4*)&lds[L_PARTB + k * 128 + 16 * wv + 4 * g];
    }
    if (tid == 0) __builtin_amdgcn_raw_buffer_store_b128(pack4(M, L, 0.f, 0.f, tg4), rWS, WS_MLB + pB * 128, 0, AUX_SC1);
    f32x4 Y[4];
#pragma unroll
    for (int cb = 0; cb < 4; ++cb) Y[cb] = f32x4{0.f, 0.f, 0.f, 0.f};
#pragma unroll
    for (int rg = 0; rg < 4; ++rg) {
      const v4u fr = andm(split_rep(o4[rg], n >= 8), mask);
#pragma unroll
      for (int cb = 0; cb < 4; ++cb) Y[cb] = mfma(wob[rg * 4 + cb], fr, Y[cb]);
    }
#pragma unroll
    for (int cb = 0; cb < 4; ++cb) {
#pragma unroll
      for (int i = 0; i < 4; ++i) Y[cb][i] += dppf<ROR8>(Y[cb][i]);
      if (n < 8) *(f32x4*)&lds[L_YPART + wv * 512 + ((cb * 4 + g) * 8 + n) * 4] = Y[cb];
    }
  }
  __syncthreads();
  if (tid < 256) {
    const int u = (wv << 5) | (lane & 31), c = 4 * u, hf = lane >> 5;
    const int idx = (((c >> 7) * 4 + ((c >> 5) & 3)) * 8 + (c & 7)) * 4 + ((c >> 3) & 3);
    float y[4] = {0.f, 0.f, 0.f, 0.f};
#pragma unroll
    for (int k = 0; k < 4; ++k)
#pragma unroll
      for (int i = 0; i < 4; ++i) y[i] += lds[L_YPART + (4 * hf + k) * 512 + idx + 4 * i];
#pragma unroll
    for (int i = 0; i < 4; ++i) y[i] = x32_sum(y[i]);
    if (lane < 32) __builtin_amdgcn_raw_buffer_store_b128(pack4(y[0], y[1], y[2], y[3], tg4), rWS, WS_YB + pB * 2048 + u * 16, 0, AUX_SC1);
  }
  {
    if (wv == 7 && !oa_done) {
      MEMFENCE();
      g2_load();
      AWAIT(g2_ok(), g2_load());
      oa_merge();
    }
    if (!ya_done) {
      while (LDS_FLAG(1) != 1u) __builtin_amdgcn_s_sleep(1);
      MEMFENCE();
      outproj64(&lds[L_OFIN], woa);
      ya_store();
    }
  }
  if (wv < 4) {
    const bool isB = w >= 128;
    const unsigned yoff = (isB ? WS_YB : WS_YA) + tid * 2048 + (w & 127) * 16;
    v4u y = ld128<AUX_SC1>(rWS, yoff, 0), ml = pack4(0.f, 1.f, 0.f, 0.f, tg4);
    if (isB) ml = ld128<AUX_SC1>(rWS, WS_MLB + tid * 128, 0);
    for (unsigned spins = 0; spins < SPIN_CAP; ++spins) {
      const bool ok = ok4(y, tg4) && ok4(ml, tg4);
      if (__all(ok)) break;
      __builtin_amdgcn_s_sleep(4);
      MEMFENCE();
      if (!ok) {
        y = ld128<AUX_SC1>(rWS, yoff, 0);
        if (isB) ml = ld128<AUX_SC1>(rWS, WS_MLB + tid * 128, 0);
      }
    }
    f32x4 v = un4(y);
    if (isB) {
      const f32x4 mlf = un4(ml);
      const float m = mlf[0];
      float M = fmaxf(m, dppf<XOR1>(m));
      M = fmaxf(M, dppf<XOR2>(M));
      M = fmaxf(M, dppf<HMIRROR>(M));
      const float f = __builtin_amdgcn_exp2f((m - M) * C2);
      float L = f * mlf[1];
      L += dppf<XOR1>(L); L += dppf<XOR2>(L); L += dppf<HMIRROR>(L);
      v *= f / L;
    }
#pragma unroll
    for (int i = 0; i < 4; ++i) {
      float t = v[i];
      t += dppf<XOR1>(t); t += dppf<XOR2>(t); t += dppf<ROR4>(t); t += dppf<ROR8>(t);
      v[i] = xrow_sum(t);
    }
    if (lane == 0) *(f32x4*)&lds[L_RED + 4 * wv] = v;
  }
  __syncthreads();
  if (tid == 0) {
    const f32x4* r = (const f32x4*)&lds[L_RED];
    const f32x4 s = (r[0] + r[1]) + (r[2] + r[3]);
    *(v2u*)(out + (w >= 128 ? 512 : 0) + 4 * (w & 127)) = v2u{cvtpk(s[0], s[1]), cvtpk(s[2], s[3])};
    if (w == 128) __builtin_amdgcn_raw_buffer_store_b32(tag, rWS, WS_GEN, 0, AUX_SC1);
  }
}

}

extern "C" void kernel_launch(void* const* d_in, const int* in_sizes, int n_in,
                              void* d_out, int out_size, void* d_ws, size_t ws_size,
                              hipStream_t stream) {
  (void)in_sizes; (void)n_in; (void)out_size; (void)ws_size;
  fused_decode<<<256, 512, 0, stream>>>(
      (const uint16_t*)d_in[0], (const uint16_t*)d_in[1], (const uint16_t*)d_in[2], (const uint16_t*)d_in[3], (const uint16_t*)d_in[4],
      (const uint16_t*)d_in[5], (const uint16_t*)d_in[6], (const uint16_t*)d_in[7], (const uint16_t*)d_in[8], (const uint16_t*)d_in[9],
      (uint16_t*)d_out, (unsigned char*)d_ws);
}
```

```cpp
#include <hip/hip_runtime.h>
#include <hip/hip_bf16.h>
#include <stdint.h>
#include <utility>

#define GAS __attribute__((address_space(1)))
typedef unsigned v4u __attribute__((ext_vector_type(4)));
typedef unsigned v2u __attribute__((ext_vector_type(2)));
typedef float f32x4 __attribute__((ext_vector_type(4)));
typedef float f32x2 __attribute__((ext_vector_type(2)));
typedef short bf16x8 __attribute__((ext_vector_type(8)));
typedef __bf16 bf16x2_t __attribute__((ext_vector_type(2)));
typedef __amdgpu_buffer_rsrc_t rsrc_t;

namespace {

constexpr int DIST = 4;
constexpr int LOCKSTEP = 2;
constexpr int K_AUX = 2, V_AUX = 2, WQ_AUX = 2, WO_AUX = 2;
constexpr int AUX_NT = 2, AUX_SC1 = 16;
constexpr int RSRC_W3 = 0x00020000;
constexpr float SCALE = 0.08838834764831845f;
constexpr float C2 = 1.4426950408889634f;
constexpr int SK = 16384, WIN0 = 16384 - 4096;
constexpr int NSLOT = 17;
constexpr int PRE = 5;
constexpr int next_unit(int u) { return (u + DIST >= PRE && u + DIST < 2 * NSLOT) ? u + DIST : -1; }
constexpr int NBUF = (DIST + 1) / 2;
constexpr int S_QA_G1 = 0, S_QA_R = S_QA_G1 + NBUF;
constexpr int S_QA_G2 = 5, S_QA_L = S_QA_G2 + NBUF;
constexpr int SA = S_QA_L + 1;
constexpr int S_PA_C = SA + 2;
constexpr int S_PA_G = 12, S_PA_R = S_PA_G + NBUF;
constexpr int S_WO = 1, WO_PER = 2, S_WOA = 13;
constexpr int S_YA = S_PA_R + 1;
static_assert(S_YA < NSLOT && S_PA_C < S_PA_G, "schedule");
constexpr unsigned SPIN_CAP = 1u << 18;

constexpr unsigned WS_GEN = 0;
constexpr unsigned WS_PQB = 4096;
constexpr unsigned WS_PQA = WS_PQB + 256 * 512;
constexpr unsigned WS_QA = WS_PQA + 256 * 512;
constexpr unsigned PA_REC = 640;
constexpr unsigned WS_PA = WS_QA + 8 * 32 * 128;
constexpr unsigned WS_MLB = WS_PA + 8 * 32 * 4 * PA_REC;
constexpr unsigned WS_YA = WS_MLB + 256 * 128;
constexpr unsigned WS_YB = WS_YA + 256 * 2048;
constexpr unsigned WS_END = WS_YB + 256 * 2048;

constexpr int L_QPART = 0;
constexpr int L_QSUM = L_QPART + 2 * 8 * 128;
constexpr int L_PARTA = L_QSUM + 8 * 128;
constexpr int L_PARTB = L_PARTA + 8 * 4 * 132;
constexpr int L_MLB = L_PARTB + 8 * 128;
constexpr int L_YPART = L_MLB + 16;
constexpr int L_RED = L_YPART + 8 * 512;
constexpr int L_OFIN = L_RED + 16;
constexpr int L_QA = L_OFIN + 16;
constexpr int L_FLAG = L_QA + 256;
constexpr int L_END = L_FLAG + 4;

__device__ __forceinline__ rsrc_t mk_rsrc(const void* p, unsigned bytes) {
  return __builtin_amdgcn_make_buffer_rsrc(const_cast<void*>(p), 0, bytes, RSRC_W3);
}
template <int AUX> __device__ __forceinline__ v4u ld128(rsrc_t rs, unsigned voff, unsigned soff) {
  return __builtin_amdgcn_raw_buffer_load_b128(rs, voff, soff, AUX);
}
__device__ __forceinline__ f32x4 mfma(v4u a, v4u b, f32x4 c) {
  return __builtin_amdgcn_mfma_f32_16x16x32_bf16(__builtin_bit_cast(bf16x8, a), __builtin_bit_cast(bf16x8, b), c, 0, 0, 0);
}
__device__ __forceinline__ unsigned cvtpk(float lo, float hi) {
  f32x2 v = {lo, hi};
  return __builtin_bit_cast(unsigned, __builtin_convertvector(v, bf16x2_t));
}
template <int CTRL> __device__ __forceinline__ float dppf(float x) {
  return __builtin_bit_cast(float, __builtin_amdgcn_mov_dpp(__builtin_bit_cast(int, x), CTRL, 0xf, 0xf, true));
}
template <int CTRL, int RM, int BM> __device__ __forceinline__ unsigned dppu(unsigned old, unsigned x) {
  return (unsigned)__builtin_amdgcn_update_dpp((int)old, (int)x, CTRL, RM, BM, false);
}
constexpr int XOR1 = 0xB1, XOR2 = 0x4E, HMIRROR = 0x141, ROR8 = 0x128, ROR4 = 0x124, BCAST = 0x150;
__device__ __forceinline__ float x16_sum(float x) {
  auto s = __builtin_amdgcn_permlane16_swap(__float_as_uint(x), __float_as_uint(x), false, false);
  return __uint_as_float(s[0]) + __uint_as_float(s[1]);
}
__device__ __forceinline__ float x32_sum(float x) {
  auto t = __builtin_amdgcn_permlane32_swap(__float_as_uint(x), __float_as_uint(x), false, false);
  return __uint_as_float(t[0]) + __uint_as_float(t[1]);
}
__device__ __forceinline__ float xrow_max(float x) {
  auto s = __builtin_amdgcn_permlane16_swap(__float_as_uint(x), __float_as_uint(x), false, false);
  x = fmaxf(__uint_as_float(s[0]), __uint_as_float(s[1]));
  auto t = __builtin_amdgcn_permlane32_swap(__float_as_uint(x), __float_as_uint(x), false, false);
  return fmaxf(__uint_as_float(t[0]), __uint_as_float(t[1]));
}
__device__ __forceinline__ float xrow_sum(float x) { return x32_sum(x16_sum(x)); }
__device__ __forceinline__ v4u andm(unsigned rep, const v4u& m) { return v4u{rep & m.x, rep & m.y, rep & m.z, rep & m.w}; }
__device__ __forceinline__ unsigned split_rep(float x, bool take_lo) {
  const unsigned hi = cvtpk(x, x);
  const float r = x - __uint_as_float(hi & 0xffff0000u);
  const unsigned lo = cvtpk(r, r);
  return take_lo ? lo : hi;
}
__device__ __forceinline__ unsigned tb(float v, unsigned byte) { return ((__float_as_uint(v) + 0x80u) & 0xffffff00u) | byte; }
__device__ __forceinline__ v4u pack4(float a, float b, float c, float d, unsigned tag) {
  return v4u{tb(a, tag & 0xffu), tb(b, (tag >> 8) & 0xffu), tb(c, (tag >> 16) & 0xffu), tb(d, tag >> 24)};
}
__device__ __forceinline__ bool ok4(const v4u& x, unsigned tag) {
  return (((x.x ^ tag) | (x.y ^ (tag >> 8)) | (x.z ^ (tag >> 16)) | (x.w ^ (tag >> 24))) & 0xffu) == 0u;
}
__device__ __forceinline__ f32x4 un4(const v4u& x) {
  return f32x4{__uint_as_float(x.x & 0xffffff00u), __uint_as_float(x.y & 0xffffff00u), __uint_as_float(x.z & 0xffffff00u),
               __uint_as_float(x.w & 0xffffff00u)};
}
__device__ __forceinline__ bool tag2(const v4u& x, unsigned tag) { return x.y == tag && x.w == tag; }

#define SB() __builtin_amdgcn_sched_barrier(0)
#define MEMFENCE() asm volatile("" ::: "memory")
#define PIN(x) asm volatile("" : "+v"(x) :: "memory")
#define AWAIT(OK, RELOAD) do { if (__builtin_expect(!(OK), 0)) { unsigned spins_ = 0; \
    do { __builtin_amdgcn_s_sleep(2); MEMFENCE(); RELOAD; } while (!(OK) && ++spins_ < SPIN_CAP); } } while (0)
#define LDS_FLAG(i) __hip_atomic_load((unsigned*)&lds[L_FLAG + (i)], __ATOMIC_RELAXED, __HIP_MEMORY_SCOPE_WORKGROUP)

template <typename F, int... I>
__device__ __forceinline__ void run_slots(F& f, std::integer_sequence<int, I...>) { (f(std::integral_constant<int, I>{}), ...); }

struct State { float m, l; f32x4 acc; };

__device__ __forceinline__ void qk_part(const v4u (&K)[4], const v4u (&Bq)[4], f32x4& S) {
  S = f32x4{0.f, 0.f, 0.f, 0.f};
#pragma unroll
  for (int u = 0; u < 4; ++u) S = mfma(K[u], Bq[u], S);
}
__device__ __forceinline__ void sm_pv(const f32x4& S, const v4u (&V)[4], const v4u& mask, int n, State& st) {
  const float s01 = (n & 1) ? S[1] : S[0], s23 = (n & 1) ? S[3] : S[2];
  float s = (n & 2) ? s23 : s01;
  s += dppf<XOR1>(s);
  s += dppf<XOR2>(s);
  s = __uint_as_float(cvtpk(s, s) & 0xffff0000u) * SCALE;
  s = __uint_as_float(cvtpk(s, s) & 0xffff0000u);
  float tm = fmaxf(s, dppf<ROR4>(s));
  tm = fmaxf(tm, dppf<ROR8>(tm));
  tm = xrow_max(tm);
  const float mn = fmaxf(st.m, tm);
  const float alpha = __builtin_amdgcn_exp2f((st.m - mn) * C2);
  const float p = __builtin_amdgcn_exp2f((s - mn) * C2);
  st.m = mn;
  st.l = fmaf(st.l, alpha, p);
  st.acc *= alpha;
  const unsigned pk = split_rep(p, (n & 2) != 0);
  unsigned rep;
#define PV_STEP(u) rep = dppu<BCAST + 4 * (u), 0xf, 0x3>(pk, pk); rep = dppu<BCAST + 4 * (u) + 2, 0xf, 0xc>(rep, pk); \
  st.acc = mfma(V[u], andm(rep, mask), st.acc);
  PV_STEP(0) PV_STEP(1) PV_STEP(2) PV_STEP(3)
#undef PV_STEP
}
__device__ __forceinline__ void finish_state(State& st) {
  float l = st.l;
  l += dppf<ROR4>(l);
  l += dppf<ROR8>(l);
  st.l = xrow_sum(l);
#pragma unroll
  for (int i = 0; i < 4; ++i) st.acc[i] += dppf<ROR8>(st.acc[i]);
}
__device__ __forceinline__ int opos(int d) { return (d >> 4) * 16 + (d & 3) * 4 + ((d >> 2) & 3); }

__global__ void __launch_bounds__(512, 2)
fused_decode(const uint16_t* __restrict__ xA, const uint16_t* __restrict__ kA, const uint16_t* __restrict__ vA,
             const uint16_t* __restrict__ wqA, const uint16_t* __restrict__ woA,
             const uint16_t* __restrict__ xB, const uint16_t* __restrict__ kB, const uint16_t* __restrict__ vB,
             const uint16_t* __restrict__ wqB, const uint16_t* __restrict__ woB,
             uint16_t* __restrict__ out, unsigned char* ws) {
  __shared__ __attribute__((aligned(16))) float lds[L_END];
  const int tid = threadIdx.x, lane = tid & 63, wv = __builtin_amdgcn_readfirstlane(tid >> 6);
  const int g = lane >> 4, n = lane & 15;
  const int w = blockIdx.x, x8 = w & 7, j = w >> 3;
  const int hB = 4 * x8 + (j & 3), sB = j >> 2;
  const int pB = hB * 8 + sB;
  const int hAq = 4 * x8 + (j >> 3), ks = j & 7;

  const rsrc_t rKB = mk_rsrc(kB + ((size_t)hB * SK + sB * 2048) * 128, 2048 * 256);
  const rsrc_t rVB = mk_rsrc(vB + ((size_t)hB * SK + sB * 2048) * 128, 2048 * 256);
  const rsrc_t rKA = mk_rsrc(kA + ((size_t)x8 * SK + WIN0 + j * 128) * 128, 128 * 256);
  const rsrc_t rVA = mk_rsrc(vA + ((size_t)x8 * SK + WIN0 + j * 128) * 128, 128 * 256);
  const rsrc_t rWQB = mk_rsrc(wqB, 512 * 8192);
  const rsrc_t rWQA = mk_rsrc(wqA, 512 * 8192);
  const rsrc_t rWOB = mk_rsrc(woB, 4096 * 1024);
  const rsrc_t rWOA = mk_rsrc(woA, 4096 * 1024);
  const rsrc_t rXB = mk_rsrc(xB, 1024);
  const rsrc_t rXA = mk_rsrc(xA, 1024);
  const rsrc_t rWS = mk_rsrc(ws, WS_END);

  const unsigned koff = (n >> 2) * 256 + g * 64 + (n & 3) * 16, voff = lane * 16;
  const unsigned wvoff = wv * 4096;
  const int rot = (w >> 3) & 15;

  v4u qa, g1, g2o[2], g2m[2], woa[2], wob[16];
  v4u wqb[2], wqa[2];
  unsigned short xbr[2], xar[2];
#pragma unroll
  for (int t = 0; t < 2; ++t) {
    wqb[t] = ld128<WQ_AUX>(rWQB, g * 8192 + n * 16, (64 * sB + 8 * wv + 4 * t) * 8192 + hB * 256);
    xbr[t] = __builtin_amdgcn_raw_buffer_load_b16(rXB, g * 2, (64 * sB + 8 * wv + 4 * t) * 2, 0);
  }
#pragma unroll
  for (int t = 0; t < 2; ++t) {
    wqa[t] = ld128<WQ_AUX>(rWQA, g * 8192 + n * 16, (64 * ks + 8 * wv + 4 * t) * 8192 + hAq * 256);
    xar[t] = __builtin_amdgcn_raw_buffer_load_b16(rXA, g * 2, (64 * ks + 8 * wv + 4 * t) * 2, 0);
  }
  const unsigned gen = __builtin_amdgcn_raw_buffer_load_b32(rWS, WS_GEN, 0, AUX_SC1);
  unsigned pt[6];
  pt[0] = __builtin_amdgcn_raw_buffer_load_b32(rKA, 0, wvoff, AUX_NT);
  pt[1] = __builtin_amdgcn_raw_buffer_load_b32(rVA, 0, wvoff, AUX_NT);
  pt[2] = __builtin_amdgcn_raw_buffer_load_b32(rWOA, 0, (512 * x8 + 16 * j) * 1024, AUX_NT);
  pt[3] = __builtin_amdgcn_raw_buffer_load_b32(rWOB, 0, (hB * 128 + 16 * wv) * 1024, AUX_NT);
  pt[4] = __builtin_amdgcn_raw_buffer_load_b32(rWS, 0, WS_YA + w * 2048, AUX_SC1);
  pt[5] = __builtin_amdgcn_raw_buffer_load_b32(rWS, 0, WS_YB + w * 2048, AUX_SC1);
  SB();
  v4u K[NSLOT][4], V[NSLOT][4];
  auto issue = [&](int u) __attribute__((always_inline)) {
    const int s = u >> 1;
    const bool isA = s == SA;
    const int tb_ = s < SA ? s : s - 1;
    if (u & 1) {
#pragma unroll
      for (int k = 0; k < 4; ++k) V[s][k] = ld128<V_AUX>(isA ? rVA : rVB, voff + k * 1024, wvoff + (isA ? 0 : ((tb_ + rot) & 15) * 32768));
    } else {
#pragma unroll
      for (int k = 0; k < 4; ++k) K[s][k] = ld128<K_AUX>(isA ? rKA : rKB, koff + k * 1024, wvoff + (isA ? 0 : ((tb_ + rot) & 15) * 32768));
    }
  };
  if (tid < 4) lds[L_FLAG + tid] = 0.f;
  __builtin_amdgcn_s_barrier();
  issue(0);
  SB();
  const unsigned tag = __builtin_amdgcn_readfirstlane(gen) + 1u;
  const unsigned tg4 = (tag & 0xffu) | (((tag + 85u) & 0xffu) << 8) | (((tag + 170u) & 0xffu) << 16) | (((tag + 37u) & 0xffu) << 24);
  v4u mask;
  {
    const unsigned hm = (n & 1) ? 0xffff0000u : 0x0000ffffu;
    const int q = (n & 7) >> 1;
    mask = v4u{q == 0 ? hm : 0u, q == 1 ? hm : 0u, q == 2 ? hm : 0u, q == 3 ? hm : 0u};
  }
  {
    f32x4 DB = {0.f, 0.f, 0.f, 0.f}, DA = {0.f, 0.f, 0.f, 0.f};
#pragma unroll
    for (int t = 0; t < 2; ++t) {
      DB = mfma(wqb[t], andm((unsigned)xbr[t] * 0x10001u, mask), DB);
      DA = mfma(wqa[t], andm((unsigned)xar[t] * 0x10001u, mask), DA);
    }
    if (n < 8) {
      *(f32x4*)&lds[L_QPART + wv * 128 + (g * 8 + n) * 4] = DB;
      *(f32x4*)&lds[L_QPART + 1024 + wv * 128 + (g * 8 + n) * 4] = DA;
    }
  }
  __syncthreads();
  if (wv < 2) {
    const int u = lane & 31, c = 4 * u, hf = lane >> 5;
    const int e = ((c >> 5) * 8 + (c & 7)) * 4 + ((c >> 3) & 3);
    float y[4] = {0.f, 0.f, 0.f, 0.f};
#pragma unroll
    for (int k = 0; k < 4; ++k)
#pragma unroll
      for (int i = 0; i < 4; ++i) y[i] += lds[L_QPART + wv * 1024 + (4 * hf + k) * 128 + e + 4 * i];
#pragma unroll
    for (int i = 0; i < 4; ++i) y[i] = x32_sum(y[i]);
    const unsigned dst = wv == 0 ? WS_PQB + pB * 512 : WS_PQA + (hAq * 8 + ks) * 512;
    if (lane < 32) __builtin_amdgcn_raw_buffer_store_b128(pack4(y[0], y[1], y[2], y[3], tg4), rWS, dst + u * 16, 0, AUX_SC1);
  }
  SB();
  issue(1);
  SB();
  PIN(K[0][3]);
  issue(2);
  SB();
  PIN(V[0][3]);
  {
    const unsigned src = WS_PQB + (hB * 8 + wv) * 512 + (lane & 31) * 16;
    v4u pq = ld128<AUX_SC1>(rWS, src, 0);
    SB();
    issue(3);
    SB();
    PIN(K[1][3]);
    issue(4);
    SB();
    AWAIT(__all(ok4(pq, tg4)), pq = ld128<AUX_SC1>(rWS, src, 0));
    if (lane < 32) *(f32x4*)&lds[L_QSUM + wv * 128 + 4 * lane] = un4(pq);
  }
  asm volatile("" :: "v"(pt[0]), "v"(pt[1]), "v"(pt[2]), "v"(pt[3]), "v"(pt[4]), "v"(pt[5]));
  __syncthreads();
  v4u Bq[4];
  {
    f32x4 a = {0.f, 0.f, 0.f, 0.f}, b = {0.f, 0.f, 0.f, 0.f};
#pragma unroll
    for (int k = 0; k < 8; ++k) {
      a += *(const f32x4*)&lds[L_QSUM + k * 128 + 8 * (4 * g + (n & 3))];
      b += *(const f32x4*)&lds[L_QSUM + k * 128 + 8 * (4 * g + (n & 3)) + 4];
    }
    const v4u qB = v4u{cvtpk(a[0], a[1]), cvtpk(a[2], a[3]), cvtpk(b[0], b[1]), cvtpk(b[2], b[3])};
#pragma unroll
    for (int u = 0; u < 4; ++u) Bq[u] = ((n >> 2) == u) ? qB : v4u{0u, 0u, 0u, 0u};
  }
  State st;
  st.m = -INFINITY; st.l = 0.f; st.acc = f32x4{0.f, 0.f, 0.f, 0.f};

  v4u ya_unit;
  auto outproj64 = [&](const float* of, const v4u (&wr)[2]) __attribute__((always_inline)) {
    f32x4 Y = {0.f, 0.f, 0.f, 0.f};
#pragma unroll
    for (int t = 0; t < 2; ++t) {
      const float o = of[8 * t + 2 * g + (n >> 3)];
      Y = mfma(wr[t], andm(split_rep(o, false), mask), Y);
      Y = mfma(wr[t], andm(split_rep(o, true), mask), Y);
    }
    const bool valid = (g >> 1) == (n >> 3);
    float z[4];
#pragma unroll
    for (int i = 0; i < 4; ++i) {
      float t = valid ? Y[i] : 0.f;
      t += dppf<ROR8>(t);
      z[i] = x32_sum(t);
    }
    const int i = 2 * (g >> 1) + (n >> 3);
    const float zi = (i & 2) ? ((i & 1) ? z[3] : z[2]) : ((i & 1) ? z[1] : z[0]);
    const float z1 = dppf<0x55>(zi), z2 = dppf<0xAA>(zi), z3 = dppf<0xFF>(zi);
    ya_unit = pack4(zi, z1, z2, z3, tg4);
  };
  const unsigned g2_base = WS_PA + ((x8 * 32 + (lane >> 2)) * 4 + (j >> 3)) * PA_REC;
  auto g2_load = [&]() __attribute__((always_inline)) {
#pragma unroll
    for (int k = 0; k < 2; ++k) {
      g2m[k] = ld128<AUX_SC1>(rWS, g2_base + k * 16 * 4 * PA_REC + 512, 0);
      g2o[k] = ld128<AUX_SC1>(rWS, g2_base + k * 16 * 4 * PA_REC + (4 * (j & 7) + (lane & 3)) * 16, 0);
    }
  };
  auto g2_ok = [&]() __attribute__((always_inline)) {
    return (bool)__all(ok4(g2m[0], tg4) && ok4(g2m[1], tg4) && ok4(g2o[0], tg4) && ok4(g2o[1], tg4));
  };
  auto oa_merge = [&]() __attribute__((always_inline)) {
    const f32x4 ml0 = un4(g2m[0]), ml1 = un4(g2m[1]);
    float M = fmaxf(ml0[0], ml1[0]);
    M = fmaxf(M, dppf<ROR4>(M));
    M = fmaxf(M, dppf<ROR8>(M));
    M = xrow_max(M);
    const float f0 = __builtin_amdgcn_exp2f((ml0[0] - M) * C2), f1 = __builtin_amdgcn_exp2f((ml1[0] - M) * C2);
    float L = f0 * ml0[1] + f1 * ml1[1];
    f32x4 a = f0 * un4(g2o[0]) + f1 * un4(g2o[1]);
    L += dppf<ROR4>(L); L += dppf<ROR8>(L); L = xrow_sum(L);
#pragma unroll
    for (int i = 0; i < 4; ++i) { a[i] += dppf<ROR4>(a[i]); a[i] += dppf<ROR8>(a[i]); a[i] = xrow_sum(a[i]); }
    if (lane < 4) *(f32x4*)&lds[L_OFIN + 4 * lane] = a * (1.f / L);
    asm volatile("s_waitcnt lgkmcnt(0)" ::: "memory");
    if (lane == 0) __hip_atomic_store((unsigned*)&lds[L_FLAG + 1], 1u, __ATOMIC_RELAXED, __HIP_MEMORY_SCOPE_WORKGROUP);
  };
  auto ya_store = [&]() __attribute__((always_inline)) {
    if (!(n & 3))
      __builtin_amdgcn_raw_buffer_store_b128(ya_unit, rWS, WS_YA + w * 2048 + (64 * wv + 32 * (g & 1) + 16 * (g >> 1) + 8 * (n >> 3) + (n & 7)) * 4, 0, AUX_SC1);
  };
  bool oa_done = false, ya_done = false;

  auto slot = [&](auto S_) __attribute__((always_inline)) {
    constexpr int s = decltype(S_)::value;
    if constexpr (s != SA) {
      f32x4 S;
      qk_part(K[s], Bq, S);
      PIN(S);
      SB();
      if constexpr (next_unit(2 * s) >= 0) issue(next_unit(2 * s));
      SB();
      sm_pv(S, V[s], mask, n, st);
      PIN(st.acc); PIN(st.l);
      SB();
      if constexpr (next_unit(2 * s + 1) >= 0) issue(next_unit(2 * s + 1));
      SB();
    } else {
      issue(next_unit(2 * s)); issue(next_unit(2 * s + 1));
      SB();
      while (LDS_FLAG(2) != 2u) __builtin_amdgcn_s_sleep(1);
      MEMFENCE();
      State sa[4];
#pragma unroll
      for (int hd = 0; hd < 4; ++hd) {
        const v4u q = *(const v4u*)&lds[L_QA + hd * 64 + (4 * g + (n & 3)) * 4];
        v4u Ba[4];
#pragma unroll
        for (int u = 0; u < 4; ++u) Ba[u] = ((n >> 2) == u) ? q : v4u{0u, 0u, 0u, 0u};
        sa[hd].m = -INFINITY; sa[hd].l = 0.f; sa[hd].acc = f32x4{0.f, 0.f, 0.f, 0.f};
        f32x4 S;
        qk_part(K[s], Ba, S);
        sm_pv(S, V[s], mask, n, sa[hd]);
        finish_state(sa[hd]);
      }
#pragma unroll
      for (int hd = 0; hd < 4; ++hd) {
        float* rec = &lds[L_PARTA + (wv * 4 + hd) * 132];
        if (n < 8) {
#pragma unroll
          for (int i = 0; i < 4; ++i) rec[4 + 32 * g + 8 * i + n] = sa[hd].acc[i];
        }
        if (lane == 0) { rec[0] = sa[hd].m; rec[1] = sa[hd].l; }
      }
      asm volatile("s_waitcnt lgkmcnt(0)" ::: "memory");
      if (lane == 0) __hip_atomic_fetch_add((unsigned*)&lds[L_FLAG], 1u, __ATOMIC_RELAXED, __HIP_MEMORY_SCOPE_WORKGROUP);
      SB();
    }
    if constexpr (s == S_PA_C) {
      while (LDS_FLAG(0) != 8u) __builtin_amdgcn_s_sleep(1);
      MEMFENCE();
      const int hd = wv >> 1, u = 16 * (wv & 1) + n;
      float M = -INFINITY;
#pragma unroll
      for (int k = 0; k < 8; ++k) M = fmaxf(M, lds[L_PARTA + (k * 4 + hd) * 132]);
      const float* r0 = &lds[L_PARTA + (2 * g * 4 + hd) * 132];
      const float* r1 = r0 + 4 * 132;
      const float f0 = __builtin_amdgcn_exp2f((r0[0] - M) * C2), f1 = __builtin_amdgcn_exp2f((r1[0] - M) * C2);
      f32x4 o = f0 * *(const f32x4*)&r0[4 + 4 * u] + f1 * *(const f32x4*)&r1[4 + 4 * u];
      const float L = xrow_sum(f0 * r0[1] + f1 * r1[1]);
#pragma unroll
      for (int i = 0; i < 4; ++i) o[i] = xrow_sum(o[i]);
      const unsigned base = WS_PA + ((x8 * 32 + j) * 4 + hd) * PA_REC;
      if (lane < 16) __builtin_amdgcn_raw_buffer_store_b128(pack4(o[0], o[1], o[2], o[3], tg4), rWS, base + u * 16, 0, AUX_SC1);
      if (lane >= 16 && lane < 24 && !(wv & 1))
        __builtin_amdgcn_raw_buffer_store_b128(pack4(M, L, 0.f, 0.f, tg4), rWS, base + 512 + (lane - 16) * 16, 0, AUX_SC1);
      SB();
    }
    if constexpr (s % LOCKSTEP == LOCKSTEP - 1 && s + 1 < NSLOT) { SB(); __builtin_amdgcn_s_barrier(); SB(); }
    if constexpr (s == S_QA_G1) if (wv == 4) {
      g1 = ld128<AUX_SC1>(rWS, WS_PQA + (hAq * 8 + ((lane >> 2) & 7)) * 512 + (4 * ks + (lane & 3)) * 16, 0);
      SB();
    }
    if constexpr (s == S_QA_R) if (wv == 4) {
      AWAIT(__all(ok4(g1, tg4)), g1 = ld128<AUX_SC1>(rWS, WS_PQA + (hAq * 8 + ((lane >> 2) & 7)) * 512 + (4 * ks + (lane & 3)) * 16, 0));
      f32x4 v = un4(g1);
#pragma unroll
      for (int i = 0; i < 4; ++i) { v[i] += dppf<ROR4>(v[i]); v[i] += dppf<ROR8>(v[i]); v[i] = x16_sum(v[i]); }
      if (lane < 4)
        __builtin_amdgcn_raw_buffer_store_b128(v4u{cvtpk(v[0], v[1]), tag, cvtpk(v[2], v[3]), tag}, rWS, WS_QA + (x8 * 32 + j) * 128 + lane * 16, 0, AUX_SC1);
      SB();
    }
    if constexpr (s == S_QA_G2) if (wv < 2) {
      qa = ld128<AUX_SC1>(rWS, WS_QA + (x8 * 32 + 16 * wv + (lane >> 2)) * 128 + (lane & 3) * 16, 0);
      SB();
    }
    if constexpr (s == S_QA_L) if (wv < 2) {
      AWAIT(__all(tag2(qa, tag)), qa = ld128<AUX_SC1>(rWS, WS_QA + (x8 * 32 + 16 * wv + (lane >> 2)) * 128 + (lane & 3) * 16, 0));
      *(v2u*)&lds[L_QA + 2 * (64 * wv + lane)] = v2u{qa.x, qa.z};
      asm volatile("s_waitcnt lgkmcnt(0)" ::: "memory");
      if (lane == 0) __hip_atomic_fetch_add((unsigned*)&lds[L_FLAG + 2], 1u, __ATOMIC_RELAXED, __HIP_MEMORY_SCOPE_WORKGROUP);
      SB();
    }
    if constexpr (s == S_PA_G) if (wv == 7) { g2_load(); SB(); }
    if constexpr (s == S_PA_R) if (wv == 7) {
      if (g2_ok()) { oa_merge(); oa_done = true; }
      SB();
    }
    if constexpr (s == S_YA) {
      if (LDS_FLAG(1) == 1u) {
        MEMFENCE();
        outproj64(&lds[L_OFIN], woa);
        ya_done = true;
      }
      SB();
    }
    if constexpr (s >= S_WO && s < S_WO + 16 / WO_PER) {
#pragma unroll
      for (int k = WO_PER * (s - S_WO); k < WO_PER * (s - S_WO) + WO_PER; ++k)
        wob[k] = ld128<WO_AUX>(rWOB, g * 1024 + n * 16, (hB * 128 + 16 * wv + 4 * (k >> 2)) * 1024 + (k & 3) * 256);
      SB();
    }
    if constexpr (s == S_WOA) {
#pragma unroll
      for (int t = 0; t < 2; ++t)
        woa[t] = ld128<AUX_NT>(rWOA, (lane >> 3) * 1024 + (lane & 7) * 16, (512 * x8 + 16 * j + 8 * t) * 1024 + wv * 128);
      SB();
    }
  };
  run_slots(slot, std::make_integer_sequence<int, NSLOT>{});

  if (ya_done) ya_store();
  finish_state(st);
  if (n < 8) {
#pragma unroll
    for (int i = 0; i < 4; ++i) lds[L_PARTB + wv * 128 + opos(32 * g + 8 * i + n)] = st.acc[i];
  }
  if (lane == 0) { lds[L_MLB + 2 * wv] = st.m; lds[L_MLB + 2 * wv + 1] = st.l; }
  __syncthreads();
  {
    float M = -INFINITY;
#pragma unroll
    for (int k = 0; k < 8; ++k) M = fmaxf(M, lds[L_MLB + 2 * k]);
    float L = 0.f;
    f32x4 o4 = {0.f, 0.f, 0.f, 0.f};
#pragma unroll
    for (int k = 0; k < 8; ++k) {
      const float f = __builtin_amdgcn_exp2f((lds[L_MLB + 2 * k] - M) * C2);
      L = fmaf(f, lds[L_MLB + 2 * k + 1], L);
      o4 += f * *(const f32x4*)&lds[L_PARTB + k * 128 + 16 * wv + 4 * g];
    }
    if (tid < 8) __builtin_amdgcn_raw_buffer_store_b128(pack4(M, L, 0.f, 0.f, tg4), rWS, WS_MLB + pB * 128 + tid * 16, 0, AUX_SC1);
    f32x4 Y[4];
#pragma unroll
    for (int cb = 0; cb < 4; ++cb) Y[cb] = f32x4{0.f, 0.f, 0.f, 0.f};
#pragma unroll
    for (int rg = 0; rg < 4; ++rg) {
      const v4u fr = andm(split_rep(o4[rg], n >= 8), mask);
#pragma unroll
      for (int cb = 0; cb < 4; ++cb) Y[cb] = mfma(wob[rg * 4 + cb], fr, Y[cb]);
    }
#pragma unroll
    for (int cb = 0; cb < 4; ++cb) {
#pragma unroll
      for (int i = 0; i < 4; ++i) Y[cb][i] += dppf<ROR8>(Y[cb][i]);
      if (n < 8) *(f32x4*)&lds[L_YPART + wv * 512 + ((cb * 4 + g) * 8 + n) * 4] = Y[cb];
    }
  }
  __syncthreads();
  if (tid < 256) {
    const int u = (wv << 5) | (lane & 31), c = 4 * u, hf = lane >> 5;
    const int idx = (((c >> 7) * 4 + ((c >> 5) & 3)) * 8 + (c & 7)) * 4 + ((c >> 3) & 3);
    float y[4] = {0.f, 0.f, 0.f, 0.f};
#pragma unroll
    for (int k = 0; k < 4; ++k)
#pragma unroll
      for (int i = 0; i < 4; ++i) y[i] += lds[L_YPART + (4 * hf + k) * 512 + idx + 4 * i];
#pragma unroll
    for (int i = 0; i < 4; ++i) y[i] = x32_sum(y[i]);
    if (lane < 32) __builtin_amdgcn_raw_buffer_store_b128(pack4(y[0], y[1], y[2], y[3], tg4), rWS, WS_YB + pB * 2048 + u * 16, 0, AUX_SC1);
  }
  {
    if (wv == 7 && !oa_done) {
      MEMFENCE();
      g2_load();
      AWAIT(g2_ok(), g2_load());
      oa_merge();
    }
    if (!ya_done) {
      while (LDS_FLAG(1) != 1u) __builtin_amdgcn_s_sleep(1);
      MEMFENCE();
      outproj64(&lds[L_OFIN], woa);
      ya_store();
    }
  }
  if (wv < 4) {
    const bool isB = w >= 128;
    const unsigned yoff = (isB ? WS_YB : WS_YA) + tid * 2048 + (w & 127) * 16;
    v4u y = ld128<AUX_SC1>(rWS, yoff, 0), ml = pack4(0.f, 1.f, 0.f, 0.f, tg4);
    if (isB) ml = ld128<AUX_SC1>(rWS, WS_MLB + tid * 128, 0);
    for (unsigned spins = 0; spins < SPIN_CAP; ++spins) {
      const bool ok = ok4(y, tg4) && ok4(ml, tg4);
      if (__all(ok)) break;
      __builtin_amdgcn_s_sleep(4);
      MEMFENCE();
      if (!ok) {
        y = ld128<AUX_SC1>(rWS, yoff, 0);
        if (isB) ml = ld128<AUX_SC1>(rWS, WS_MLB + tid * 128, 0);
      }
    }
    f32x4 v = un4(y);
    if (isB) {
      const f32x4 mlf = un4(ml);
      const float m = mlf[0];
      float M = fmaxf(m, dppf<XOR1>(m));
      M = fmaxf(M, dppf<XOR2>(M));
      M = fmaxf(M, dppf<HMIRROR>(M));
      const float f = __builtin_amdgcn_exp2f((m - M) * C2);
      float L = f * mlf[1];
      L += dppf<XOR1>(L); L += dppf<XOR2>(L); L += dppf<HMIRROR>(L);
      v *= f / L;
    }
#pragma unroll
    for (int i = 0; i < 4; ++i) {
      float t = v[i];
      t += dppf<XOR1>(t); t += dppf<XOR2>(t); t += dppf<ROR4>(t); t += dppf<ROR8>(t);
      v[i] = xrow_sum(t);
    }
    if (lane == 0) *(f32x4*)&lds[L_RED + 4 * wv] = v;
  }
  __syncthreads();
  if (tid == 0) {
    const f32x4* r = (const f32x4*)&lds[L_RED];
    const f32x4 s = (r[0] + r[1]) + (r[2] + r[3]);
    *(v2u*)(out + (w >= 128 ? 512 : 0) + 4 * (w & 127)) = v2u{cvtpk(s[0], s[1]), cvtpk(s[2], s[3])};
    if (w == 128) __builtin_amdgcn_raw_buffer_store_b32(tag, rWS, WS_GEN, 0, AUX_SC1);
  }
}

}

extern "C" void kernel_launch(void* const* d_in, const int* in_sizes, int n_in,
                              void* d_out, int out_size, void* d_ws, size_t ws_size,
                              hipStream_t stream) {
  (void)in_sizes; (void)n_in; (void)out_size; (void)ws_size;
  fused_decode<<<256, 512, 0, stream>>>(
      (const uint16_t*)d_in[0], (const uint16_t*)d_in[1], (const uint16_t*)d_in[2], (const uint16_t*)d_in[3], (const uint16_t*)d_in[4],
      (const uint16_t*)d_in[5], (const uint16_t*)d_in[6], (const uint16_t*)d_in[7], (const uint16_t*)d_in[8], (const uint16_t*)d_in[9],
      (uint16_t*)d_out, (unsigned char*)d_ws);
}
```
